```python
import torch

def _softmax(x, axis=-1):
    return torch.softmax(x, dim=axis)

def _gelu(x):
    return torch.nn.functional.gelu(x, approximate='tanh')

def _silu(x):
    return torch.nn.functional.silu(x)

def _sigmoid(x):
    return torch.sigmoid(x)

def _layernorm(x, axis=-1, eps=1e-05):
    m = x.mean(dim=axis, keepdim=True)
    v = x.var(dim=axis, keepdim=True, unbiased=False)
    return (x - m) / torch.sqrt(v + eps)

def _scatter_add(tgt, idx, src, axis=0):
    return tgt.clone().index_add_(axis, idx, src)

def _segment_sum(src, seg, n):
    out = torch.zeros((n,) + src.shape[1:], dtype=src.dtype)
    return out.index_add_(0, seg, src)

def _pad(x, pw):
    flat = tuple((v for pair in reversed(pw) for v in pair))
    return torch.nn.functional.pad(x, flat)
kernel_name = 'gated_mix__spectral_gfnet__memory_aug_recurrent_mem_tr__prompt'

def setup_inputs():

    _w = lambda a: (lambda t: t.to(torch.bfloat16) if t.is_floating_point() else t)(torch.as_tensor(a))

    def _A_setup():
        B, S, D = (1, 2048, 640)
        Df = D // 2 + 1
        return {'x': _w((lambda *s: torch.randn(*s, dtype=torch.bfloat16) * 0.05)(B, S, D)), 'w_r': _w((lambda *s: torch.randn(*s, dtype=torch.bfloat16) * 3.2)(S, Df) * 0.02), 'w_i': _w((lambda *s: torch.randn(*s, dtype=torch.bfloat16) * 3.2)(S, Df) * 0.02)}
    _A_in = _A_setup()

    def _B_setup():
        B, S, Dm = (1, 2048, 640)
        H, Dh = (32, 128)
        M, seg = (8, 32)
        seg = min(seg, S)
        S = S // seg * seg
        return {'x': _w((lambda *s: torch.randn(*s, dtype=torch.bfloat16) * 0.05)(B, S, Dm)), 'mem0': _w((lambda *s: torch.randn(*s, dtype=torch.bfloat16) * 0.4)(B, M, Dm)), 'Wq': _w((lambda *s: torch.randn(*s, dtype=torch.bfloat16) * 6.4)(Dm, H, Dh) * Dm ** (-0.5)), 'Wk': _w((lambda *s: torch.randn(*s, dtype=torch.bfloat16) * 6.4)(Dm, H, Dh) * Dm ** (-0.5)), 'Wv': _w((lambda *s: torch.randn(*s, dtype=torch.bfloat16) * 0.4)(Dm, H, Dh) * Dm ** (-0.5)), 'Wo': _w((lambda *s: torch.randn(*s, dtype=torch.bfloat16) * 0.4)(H, Dh, Dm) * (H * Dh) ** (-0.5)), 'seg_len': seg}
    _B_in = _B_setup()
    return {'A_x': _A_in['x'], 'A_w_r': _A_in['w_r'], 'A_w_i': _A_in['w_i'], 'B_x': _B_in['x'], 'B_mem0': _B_in['mem0'], 'B_Wq': _B_in['Wq'], 'B_Wk': _B_in['Wk'], 'B_Wv': _B_in['Wv'], 'B_Wo': _B_in['Wo'], 'B_seg_len': _B_in['seg_len'], 'gate_w': _w((lambda *s: torch.randn(*s, dtype=torch.bfloat16) * 0.05)(640) * 16.0)}

def reference(A_x, A_w_r, A_w_i, B_x, B_mem0, B_Wq, B_Wk, B_Wv, B_Wo, B_seg_len, gate_w):

    def _A_ref(x, w_r, w_i):
        B, S, D = x.shape
        X = (lambda x, axes=(-2, -1): torch.fft.rfft2(x.float(), dim=axes))(x, axes=(-2, -1))
        W = w_r + 1j * w_i
        X = X * W
        y = (lambda x, s, axes=(-2, -1): torch.fft.irfft2(x, s=s, dim=axes).to(torch.bfloat16))(X, (S, D), axes=(-2, -1))
        return y

    def _B_ref(x, mem0, Wq, Wk, Wv, Wo, seg_len):
        B, S, Dm = x.shape
        H, Dh = (Wq.shape[1], Wq.shape[2])
        M = mem0.shape[1]
        scale = Dh ** (-0.5)
        n_seg = S // seg_len
        L = M + seg_len
        idx = torch.arange(L)
        mask = (idx[:, None] < M) | (idx[:, None] >= idx[None, :])
        mem = mem0
        outs = []
        for s in range(n_seg):
            xs = x[:, s * seg_len:(s + 1) * seg_len, :].clone()
            h = torch.concatenate([mem, xs], axis=1)
            q = torch.einsum('btd,dhe->bhte', h, Wq)
            k = torch.einsum('btd,dhe->bhte', h, Wk)
            v = torch.einsum('btd,dhe->bhte', h, Wv)
            scores = torch.einsum('bhqe,bhke->bhqk', q, k) * scale
            scores = torch.where(mask[None, None], scores, -1000000000.0)
            p = _softmax(scores, axis=-1)
            o = torch.einsum('bhqk,bhke->bhqe', p, v)
            o = torch.einsum('bhte,hed->btd', o, Wo)
            mem = o[:, :M, :].clone()
            outs.append(o[:, M:, :])
        return torch.concatenate(outs, axis=1)
    x = A_x
    g = _sigmoid((x * gate_w).sum(-1, keepdims=True))
    a = _A_ref(x, A_w_r, A_w_i)
    b = _B_ref(x, B_mem0, B_Wq, B_Wk, B_Wv, B_Wo, B_seg_len)
    return g * a + (1 - g) * b
```

```cpp
#include <hip/hip_runtime.h>
#include <hip/hip_bf16.h>
#include <stdint.h>

typedef unsigned short u16;
__device__ __forceinline__ float bf2f(u16 v) { return __uint_as_float(((unsigned)v) << 16); }
__device__ __forceinline__ u16 f2bf(float f) { __hip_bfloat16 b = __float2bfloat16(f); return *reinterpret_cast<u16*>(&b); }
__device__ __forceinline__ float rbf(float f) { return bf2f(f2bf(f)); }

constexpr int S = 2048, D = 640, DF = 321, H = 32, DH = 128, HD = 4096, M = 8, SEG = 32, NSEG = 64, L = 40;

__global__ void gemm_naive(const u16* __restrict__ A, const u16* __restrict__ B, u16* __restrict__ C, int Mr, int N, int K) {
    __shared__ float As[32][33], Bs[32][33];
    int tx = threadIdx.x & 31, ty = threadIdx.x >> 5;
    int n0 = blockIdx.x * 32, m0 = blockIdx.y * 32;
    float acc[4] = {0, 0, 0, 0};
    for (int k0 = 0; k0 < K; k0 += 32) {
        for (int r = ty; r < 32; r += 8) {
            int m = m0 + r;
            As[r][tx] = m < Mr ? bf2f(A[(size_t)m * K + k0 + tx]) : 0.f;
            Bs[r][tx] = bf2f(B[(size_t)(k0 + r) * N + n0 + tx]);
        }
        __syncthreads();
        for (int k = 0; k < 32; ++k) {
            float b = Bs[k][tx];
            for (int j = 0; j < 4; ++j) acc[j] += As[ty * 4 + j][k] * b;
        }
        __syncthreads();
    }
    for (int j = 0; j < 4; ++j) { int m = m0 + ty * 4 + j; if (m < Mr) C[(size_t)m * N + n0 + tx] = f2bf(acc[j]); }
}

__global__ void attn_naive(const u16* Qm, const u16* Km, const u16* Vm, const u16* Qx, const u16* Kx, const u16* Vx, u16* Om, u16* Ox, int s) {
    int h = blockIdx.x;
    __shared__ float q[L][DH], k[L][DH], v[L][DH], p[L][L];
    for (int i = threadIdx.x; i < L * DH; i += blockDim.x) {
        int r = i / DH, e = i % DH;
        if (r < M) { q[r][e] = bf2f(Qm[r * HD + h * DH + e]); k[r][e] = bf2f(Km[r * HD + h * DH + e]); v[r][e] = bf2f(Vm[r * HD + h * DH + e]); }
        else { size_t t = (size_t)(s * SEG + r - M) * HD + h * DH + e; q[r][e] = bf2f(Qx[t]); k[r][e] = bf2f(Kx[t]); v[r][e] = bf2f(Vx[t]); }
    }
    __syncthreads();
    const float scale = 0.08838834764831845f;
    for (int i = threadIdx.x; i < L * L; i += blockDim.x) {
        int a = i / L, b = i % L;
        float acc = 0; for (int e = 0; e < DH; ++e) acc += q[a][e] * k[b][e];
        float sc = rbf(rbf(acc) * scale);
        bool mask = (a < M) || (a >= b);
        p[a][b] = mask ? sc : rbf(-1e9f);
    }
    __syncthreads();
    if (threadIdx.x < L) {
        int a = threadIdx.x; float mx = -INFINITY; for (int b = 0; b < L; ++b) mx = fmaxf(mx, p[a][b]);
        float sum = 0; for (int b = 0; b < L; ++b) { float e = expf(p[a][b] - mx); p[a][b] = e; sum += e; }
        for (int b = 0; b < L; ++b) p[a][b] = rbf(p[a][b] / sum);
    }
    __syncthreads();
    for (int i = threadIdx.x; i < L * DH; i += blockDim.x) {
        int a = i / DH, e = i % DH;
        float acc = 0; for (int b = 0; b < L; ++b) acc += p[a][b] * v[b][e];
        if (a < M) Om[a * HD + h * DH + e] = f2bf(acc); else Ox[(size_t)(s * SEG + a - M) * HD + h * DH + e] = f2bf(acc);
    }
}

__global__ void dft_d_fwd(const u16* x, float2* X1) {
    int s = blockIdx.x; __shared__ float xs[D], cs[D], sn[D];
    for (int i = threadIdx.x; i < D; i += blockDim.x) { xs[i] = bf2f(x[s * D + i]); float c, sv; sincospif(2.0f * i / D, &sv, &c); cs[i] = c; sn[i] = sv; }
    __syncthreads();
    for (int f = threadIdx.x; f < DF; f += blockDim.x) {
        float re = 0, im = 0; int idx = 0;
        for (int d = 0; d < D; ++d) { re += xs[d] * cs[idx]; im -= xs[d] * sn[idx]; idx += f; if (idx >= D) idx -= D; }
        X1[s * DF + f] = make_float2(re, im);
    }
}
__global__ void dft_s(const float2* in, float2* out, const u16* wr, const u16* wi, int dir) {
    int u = blockIdx.x; __shared__ float cs[S], sn[S];
    for (int i = threadIdx.x; i < S; i += blockDim.x) { float c, sv; sincospif(2.0f * i / S, &sv, &c); cs[i] = c; sn[i] = sv * dir; }
    __syncthreads();
    for (int f = threadIdx.x; f < DF; f += blockDim.x) {
        float re = 0, im = 0; int idx = 0;
        for (int s = 0; s < S; ++s) { float2 v = in[s * DF + f]; float c = cs[idx], sv = sn[idx]; re += v.x * c - v.y * sv; im += v.x * sv + v.y * c; idx = (idx + u) & (S - 1); }
        if (dir < 0) { float a = bf2f(wr[u * DF + f]), b = bf2f(wi[u * DF + f]); out[u * DF + f] = make_float2(re * a - im * b, re * b + im * a); }
        else out[u * DF + f] = make_float2(re / S, im / S);
    }
}
__global__ void final_k(const float2* Y1, const u16* x, const u16* gw, const u16* Bo, u16* out) {
    int s = blockIdx.x; __shared__ float cs[D], sn[D]; __shared__ float2 y[DF]; __shared__ float red[256];
    for (int i = threadIdx.x; i < D; i += blockDim.x) { float c, sv; sincospif(2.0f * i / D, &sv, &c); cs[i] = c; sn[i] = sv; }
    for (int i = threadIdx.x; i < DF; i += blockDim.x) y[i] = Y1[s * DF + i];
    float part = 0; for (int i = threadIdx.x; i < D; i += blockDim.x) part += rbf(bf2f(x[s * D + i]) * bf2f(gw[i]));
    red[threadIdx.x] = part; __syncthreads();
    for (int o = 128; o > 0; o >>= 1) { if (threadIdx.x < o) red[threadIdx.x] += red[threadIdx.x + o]; __syncthreads(); }
    float z = rbf(red[0]); float g = rbf(1.0f / (1.0f + expf(-z)));
    float omg = rbf(1.0f - g);
    for (int d = threadIdx.x; d < D; d += blockDim.x) {
        float acc = y[0].x + ((d & 1) ? -y[320].x : y[320].x); float a2 = 0; int idx = d;
        for (int f = 1; f < 320; ++f) { a2 += y[f].x * cs[idx] - y[f].y * sn[idx]; idx += d; if (idx >= D) idx -= D; }
        float a = rbf((acc + 2.0f * a2) / D);
        float b = bf2f(Bo[s * D + d]);
        out[s * D + d] = f2bf(rbf(g * a) + rbf(omg * b));
    }
}

extern "C" void kernel_launch(void* const* d_in, const int* in_sizes, int n_in, void* d_out, int out_size, void* d_ws, size_t ws_size, hipStream_t stream) {
    const u16* x = (const u16*)d_in[0]; const u16* wr = (const u16*)d_in[1]; const u16* wi = (const u16*)d_in[2];
    const u16* mem0 = (const u16*)d_in[4]; const u16* Wq = (const u16*)d_in[5]; const u16* Wk = (const u16*)d_in[6]; const u16* Wv = (const u16*)d_in[7]; const u16* Wo = (const u16*)d_in[8];
    const u16* gw = (const u16*)d_in[10];
    char* ws = (char*)d_ws; size_t off = 0;
    auto alloc = [&](size_t n) { char* p = ws + off; off += (n + 255) & ~(size_t)255; return p; };
    u16* Qx = (u16*)alloc((size_t)S * HD * 2); u16* Kx = (u16*)alloc((size_t)S * HD * 2); u16* Vx = (u16*)alloc((size_t)S * HD * 2); u16* Ox = (u16*)alloc((size_t)S * HD * 2);
    u16* Qm = (u16*)alloc(M * HD * 2); u16* Km = (u16*)alloc(M * HD * 2); u16* Vm = (u16*)alloc(M * HD * 2); u16* Om = (u16*)alloc(M * HD * 2);
    u16* mem = (u16*)alloc(M * D * 2); u16* Bo = (u16*)alloc((size_t)S * D * 2);
    float2* X1 = (float2*)alloc((size_t)S * DF * 8); float2* X2 = (float2*)alloc((size_t)S * DF * 8);
    gemm_naive<<<dim3(HD / 32, S / 32), 256, 0, stream>>>(x, Wq, Qx, S, HD, D);
    gemm_naive<<<dim3(HD / 32, S / 32), 256, 0, stream>>>(x, Wk, Kx, S, HD, D);
    gemm_naive<<<dim3(HD / 32, S / 32), 256, 0, stream>>>(x, Wv, Vx, S, HD, D);
    for (int s = 0; s < NSEG; ++s) {
        const u16* m = s == 0 ? mem0 : mem;
        gemm_naive<<<dim3(HD / 32, 1), 256, 0, stream>>>(m, Wq, Qm, M, HD, D);
        gemm_naive<<<dim3(HD / 32, 1), 256, 0, stream>>>(m, Wk, Km, M, HD, D);
        gemm_naive<<<dim3(HD / 32, 1), 256, 0, stream>>>(m, Wv, Vm, M, HD, D);
        attn_naive<<<H, 256, 0, stream>>>(Qm, Km, Vm, Qx, Kx, Vx, Om, Ox, s);
        gemm_naive<<<dim3(D / 32, 1), 256, 0, stream>>>(Om, Wo, mem, M, D, HD);
    }
    gemm_naive<<<dim3(D / 32, S / 32), 256, 0, stream>>>(Ox, Wo, Bo, S, D, HD);
    dft_d_fwd<<<S, 256, 0, stream>>>(x, X1);
    dft_s<<<S, 256, 0, stream>>>(X1, X2, wr, wi, -1);
    dft_s<<<S, 256, 0, stream>>>(X2, X1, wr, wi, 1);
    final_k<<<S, 256, 0, stream>>>(X1, x, gw, Bo, (u16*)d_out);
}
```

```cpp
constexpr unsigned O_FF = O_FFT;
constexpr unsigned O_GF = O_FF + 40 * 20 * 2 * 1024;
constexpr unsigned O_WT = O_GF + 40 * 20 * 2 * 1024;
constexpr unsigned O_XT = O_WT + 324 * 2048 * 4;
constexpr unsigned O_YH = O_XT + 640 * 2048 * 4;
constexpr unsigned O_YL = O_YH + 640 * 2048 * 2;
constexpr unsigned O_FFT_END = O_YL + 640 * 2048 * 2;

#define split2(a, b, hi, lo) do { unsigned h_ = pk2(a, b); hi = h_; lo = pk2((a) - l16f(h_), (b) - h16f(h_)); } while (0)

__device__ __forceinline__ void wave_dftmat(const WS& ws, int item) {
    const int lane = otid() & 63, g = lane >> 4, c = lane & 15;
    const int which = item / 800, tile = (item % 800) / 20, k = item % 20;
    const rsrc_t rw = ws_rsrc(ws);
    float v[8];
#pragma unroll
    for (int j = 0; j < 8; ++j) {
        int n, d;
        if (which == 0) { n = 16 * tile + c; d = 32 * k + 8 * g + j; }
        else { n = 32 * k + 8 * g + j; d = 16 * tile + c; }
        const bool im = n > 320; const int f = im ? n - 320 : n;
        const int idx = (f * d) % D;
        float sn, cs; sincospif((float)idx * (2.0f / D), &sn, &cs);
        float val = im ? -sn : cs;
        if (which == 1 && f != 0 && f != 320) val *= 2.0f;
        v[j] = val;
    }
    v4u hi, lo;
    split2(v[0], v[1], hi.x, lo.x); split2(v[2], v[3], hi.y, lo.y); split2(v[4], v[5], hi.z, lo.z); split2(v[6], v[7], hi.w, lo.w);
    const unsigned o = (which ? O_GF : O_FF) + (unsigned)((tile * 20 + k) * 2) * 1024 + lane * 16;
    st16(rw, o, hi); st16(rw, o + 1024, lo);
}

__device__ __forceinline__ void task_wt(const WS& ws, int fb, LAS unsigned char* lds) {
    const int tid = otid();
    const rsrc_t rw = ws_rsrc(ws);
    LAS unsigned* tile = (LAS unsigned*)lds;
    const int f0 = fb * 16;
    for (int q = tid; q < 2048 * 16; q += 512) {
        const int u = q >> 4, fl = q & 15, f = f0 + fl;
        unsigned v = 0;
        if (f < DF) v = (unsigned)ws.wr[u * DF + f] | ((unsigned)ws.wi[u * DF + f] << 16);
        const int k0 = u & 15, k1 = (u >> 4) & 15, k2 = u >> 8, p = k0 * 16 + k1, t = p & 127, half = p >> 7;
        tile[fl * 2052 + t * 16 + half * 8 + k2] = v;
    }
    __syncthreads();
    for (int q = tid; q < 16 * 512; q += 512) {
        const int fl = q >> 9, ch = q & 511;
        if (f0 + fl < 324) st16(rw, O_WT + (unsigned)(f0 + fl) * 8192 + ch * 16, *(LAS v4u*)(tile + fl * 2052 + ch * 4));
    }
    __syncthreads();
}

__device__ __forceinline__ void task_f1(const WS& ws, int b, int cb) {
    const int tid = otid(), lane = tid & 63, w = __builtin_amdgcn_readfirstlane(tid >> 6);
    const int g = lane >> 4, c = lane & 15;
    const rsrc_t rw = ws_rsrc(ws);
    const int th = w & 1, cg = w >> 1;
    f32x4 acc[4][2];
#pragma unroll
    for (int t = 0; t < 4; ++t) { acc[t][0] = (f32x4){0, 0, 0, 0}; acc[t][1] = (f32x4){0, 0, 0, 0}; }
    const u16* xa = ws.x + (size_t)(b * 128 + th * 64 + c) * D + 8 * g;
    const unsigned fo = O_FF + (unsigned)(cb * 8 + cg * 2) * 40960 + lane * 16;
#pragma unroll 2
    for (int k = 0; k < 20; ++k) {
        v4u a[4], bh[2], bl[2];
#pragma unroll
        for (int t = 0; t < 4; ++t) a[t] = *(const v4u*)(xa + (size_t)t * 16 * D + k * 32);
#pragma unroll
        for (int n = 0; n < 2; ++n) { bh[n] = ld16(rw, fo + n * 40960 + k * 2048); bl[n] = ld16(rw, fo + n * 40960 + k * 2048 + 1024); }
#pragma unroll
        for (int t = 0; t < 4; ++t)
#pragma unroll
            for (int n = 0; n < 2; ++n) {
                acc[t][n] = __builtin_amdgcn_mfma_f32_16x16x32_bf16(__builtin_bit_cast(bf16x8, a[t]), __builtin_bit_cast(bf16x8, bh[n]), acc[t][n], 0, 0, 0);
                acc[t][n] = __builtin_amdgcn_mfma_f32_16x16x32_bf16(__builtin_bit_cast(bf16x8, a[t]), __builtin_bit_cast(bf16x8, bl[n]), acc[t][n], 0, 0, 0);
            }
    }
#pragma unroll
    for (int t = 0; t < 4; ++t)
#pragma unroll
        for (int n = 0; n < 2; ++n) {
            const int col = cb * 128 + (cg * 2 + n) * 16 + c, s0 = b * 128 + th * 64 + t * 16 + 4 * g;
            st16(rw, O_XT + (unsigned)(col * 2048 + s0) * 4u, __builtin_bit_cast(v4u, acc[t][n]));
        }
}

struct cf { float x, y; };
__device__ __forceinline__ cf cadd(cf a, cf b) { return {a.x + b.x, a.y + b.y}; }
__device__ __forceinline__ cf csub(cf a, cf b) { return {a.x - b.x, a.y - b.y}; }
__device__ __forceinline__ cf cmul(cf a, cf b) { return {a.x * b.x - a.y * b.y, a.x * b.y + a.y * b.x}; }
template <int DIR> __device__ __forceinline__ cf mulI(cf a) { return DIR < 0 ? cf{a.y, -a.x} : cf{-a.y, a.x}; }
template <int DIR> __device__ __forceinline__ void dft4(cf& a, cf& b, cf& c, cf& d) {
    cf s0 = cadd(a, c), s1 = csub(a, c), s2 = cadd(b, d), s3 = mulI<DIR>(csub(b, d));
    a = cadd(s0, s2); c = csub(s0, s2); b = cadd(s1, s3); d = csub(s1, s3);
}
template <int DIR> __device__ __forceinline__ cf tw16(cf a, int k) {
    const float C1 = 0.92387953251128674f, S1 = 0.38268343236508977f, R = 0.70710678118654752f;
    const float cs[10] = {1.f, C1, R, S1, 0.f, -S1, -R, -C1, -1.f, -C1}, sn[10] = {0.f, S1, R, C1, 1.f, C1, R, S1, 0.f, -S1};
    if (k == 0) return a;
    const float c = cs[k], s = DIR * sn[k];
    return {a.x * c - a.y * s, a.x * s + a.y * c};
}
template <int DIR> __device__ __forceinline__ void dft16(cf (&v)[16]) {
#pragma unroll
    for (int i = 0; i < 4; ++i) dft4<DIR>(v[i], v[i + 4], v[i + 8], v[i + 12]);
#pragma unroll
    for (int i = 1; i < 4; ++i)
#pragma unroll
        for (int k1 = 1; k1 < 4; ++k1) v[i + 4 * k1] = tw16<DIR>(v[i + 4 * k1], i * k1);
#pragma unroll
    for (int k1 = 0; k1 < 4; ++k1) dft4<DIR>(v[4 * k1], v[4 * k1 + 1], v[4 * k1 + 2], v[4 * k1 + 3]);
#pragma unroll
    for (int a = 0; a < 4; ++a)
#pragma unroll
        for (int b = a + 1; b < 4; ++b) { cf t = v[4 * a + b]; v[4 * a + b] = v[4 * b + a]; v[4 * b + a] = t; }
}
template <int DIR> __device__ __forceinline__ void dft8(cf* v) {
    dft4<DIR>(v[0], v[2], v[4], v[6]); dft4<DIR>(v[1], v[3], v[5], v[7]);
    v[3] = tw16<DIR>(v[3], 2); v[5] = tw16<DIR>(v[5], 4); v[7] = tw16<DIR>(v[7], 6);
    cf o[8];
#pragma unroll
    for (int k1 = 0; k1 < 4; ++k1) { o[k1] = cadd(v[2 * k1], v[2 * k1 + 1]); o[k1 + 4] = csub(v[2 * k1], v[2 * k1 + 1]); }
#pragma unroll
    for (int i = 0; i < 8; ++i) v[i] = o[i];
}
__device__ __forceinline__ int fpad(int i) { return i + (i >> 4); }
__device__ __forceinline__ void task_f2(const WS& ws, int fg, LAS unsigned char* lds, bool make_table) {
    const int tid = otid(), col = tid >> 7, t = tid & 127;
    const rsrc_t rw = ws_rsrc(ws);
    LAS f32x2v* tw = (LAS f32x2v*)lds;
    LAS f32x2v* buf = (LAS f32x2v*)(lds + 16384) + col * 2176;
    if (make_table) {
        for (int i = tid; i < 2048; i += 512) { float sn, cs; sincospif((float)i * (1.0f / 1024.0f), &sn, &cs); tw[i] = (f32x2v){cs, sn}; }
    }
    const int f = fg * 4 + col;
    const bool valid = f < DF, hasim = valid && f != 0 && f != 320;
    const int fr = valid ? f : 0;
    cf v[16];
#pragma unroll
    for (int s2 = 0; s2 < 16; ++s2) {
        v[s2].x = __uint_as_float(__builtin_amdgcn_raw_buffer_load_b32(rw, O_XT + (unsigned)(fr * 2048 + s2 * 128 + t) * 4u, 0, 16));
        v[s2].y = hasim ? __uint_as_float(__builtin_amdgcn_raw_buffer_load_b32(rw, O_XT + (unsigned)((320 + fr) * 2048 + s2 * 128 + t) * 4u, 0, 16)) : 0.f;
    }
    v4u wv[4];
#pragma unroll
    for (int i = 0; i < 4; ++i) wv[i] = ld16(rw, O_WT + (unsigned)fr * 8192 + t * 64 + i * 16);
    __syncthreads();
    dft16<-1>(v);
#pragma unroll
    for (int k0 = 1; k0 < 16; ++k0) { f32x2v w = tw[k0 * t]; v[k0] = cmul(v[k0], cf{w.x, -w.y}); }
#pragma unroll
    for (int k0 = 0; k0 < 16; ++k0) buf[fpad(k0 * 128 + t)] = (f32x2v){v[k0].x, v[k0].y};
    __syncthreads();
    { const int k0 = t >> 3, s0 = t & 7;
#pragma unroll
      for (int s1 = 0; s1 < 16; ++s1) { f32x2v q = buf[fpad(k0 * 128 + s1 * 8 + s0)]; v[s1] = cf{q.x, q.y}; }
      dft16<-1>(v);
#pragma unroll
      for (int k1 = 1; k1 < 16; ++k1) { f32x2v w = tw[k1 * s0 * 16]; v[k1] = cmul(v[k1], cf{w.x, -w.y}); }
      __syncthreads();
#pragma unroll
      for (int k1 = 0; k1 < 16; ++k1) buf[fpad((k0 * 16 + k1) * 8 + s0)] = (f32x2v){v[k1].x, v[k1].y};
    }
    __syncthreads();
#pragma unroll
    for (int hf = 0; hf < 2; ++hf) {
#pragma unroll
        for (int s0 = 0; s0 < 8; ++s0) { f32x2v q = buf[fpad((t + 128 * hf) * 8 + s0)]; v[hf * 8 + s0] = cf{q.x, q.y}; }
        dft8<-1>(v + hf * 8);
#pragma unroll
        for (int k2 = 0; k2 < 8; ++k2) { const unsigned ww = wv[hf * 2 + (k2 >> 2)][k2 & 3]; v[hf * 8 + k2] = cmul(v[hf * 8 + k2], cf{l16f(ww), h16f(ww)}); }
        dft8<1>(v + hf * 8);
#pragma unroll
        for (int s0 = 0; s0 < 8; ++s0) buf[fpad((t + 128 * hf) * 8 + s0)] = (f32x2v){v[hf * 8 + s0].x, v[hf * 8 + s0].y};
    }
    __syncthreads();
    { const int k0 = t >> 3, s0 = t & 7;
#pragma unroll
      for (int k1 = 0; k1 < 16; ++k1) { f32x2v q = buf[fpad((k0 * 16 + k1) * 8 + s0)]; v[k1] = cf{q.x, q.y}; }
#pragma unroll
      for (int k1 = 1; k1 < 16; ++k1) { f32x2v w = tw[k1 * s0 * 16]; v[k1] = cmul(v[k1], cf{w.x, w.y}); }
      dft16<1>(v);
      __syncthreads();
#pragma unroll
      for (int s1 = 0; s1 < 16; ++s1) buf[fpad(k0 * 128 + s1 * 8 + s0)] = (f32x2v){v[s1].x, v[s1].y};
    }
    __syncthreads();
#pragma unroll
    for (int k0 = 0; k0 < 16; ++k0) { f32x2v q = buf[fpad(k0 * 128 + t)]; v[k0] = cf{q.x, q.y}; }
#pragma unroll
    for (int k0 = 1; k0 < 16; ++k0) { f32x2v w = tw[k0 * t]; v[k0] = cmul(v[k0], cf{w.x, w.y}); }
    dft16<1>(v);
    __syncthreads();
#pragma unroll
    for (int s2 = 0; s2 < 16; ++s2) buf[fpad(s2 * 128 + t)] = (f32x2v){v[s2].x, v[s2].y};
    __syncthreads();
#pragma unroll
    for (int hf = 0; hf < 2; ++hf) {
        const int s0 = 8 * (t + 128 * hf);
        f32x2v q[8];
#pragma unroll
        for (int i = 0; i < 8; ++i) q[i] = buf[fpad(s0 + i)];
        v4u rh, rl, ih, il;
        split2(q[0].x, q[1].x, rh.x, rl.x); split2(q[2].x, q[3].x, rh.y, rl.y); split2(q[4].x, q[5].x, rh.z, rl.z); split2(q[6].x, q[7].x, rh.w, rl.w);
        split2(q[0].y, q[1].y, ih.x, il.x); split2(q[2].y, q[3].y, ih.y, il.y); split2(q[4].y, q[5].y, ih.z, il.z); split2(q[6].y, q[7].y, ih.w, il.w);
        const unsigned yo = (unsigned)(((s0 >> 5) * 640 + f) * 32 + (s0 & 31)) * 2u;
        if (valid) { st16(rw, O_YH + yo, rh); st16(rw, O_YL + yo, rl); }
        if (hasim) { st16(rw, O_YH + yo + 320 * 64, ih); st16(rw, O_YL + yo + 320 * 64, il); }
    }
    __syncthreads();
}

__device__ __forceinline__ void stage_x(const WS& ws, int s, LAS unsigned char* dst, int w, int lane) {
#pragma unroll
    for (int i = 0; i < 5; ++i) {
        const int pc = w * 5 + i, q = pc * 64 + lane, row = q / 80, slot = q - row * 80;
        glds16_asm(ws.x + (size_t)(s * 32 + row) * D + (slot ^ (row & 15)) * 8, dst + pc * 1024);
    }
}
__device__ __forceinline__ bf16x8 xfrag(LAS unsigned char* xt, int tt, int k, int c, int g) {
    return __builtin_bit_cast(bf16x8, *(LAS v4u*)(xt + (c * 1280 + (((4 * (k & 3) + g) ^ c) << 4)) + (tt * 20480 + (k >> 2) * 256)));
}

#define XPIPE(xt, BODY, NB) do { \
    bf16x8 xb_[2][2 * NB]; \
    _Pragma("unroll") for (int q_ = 0; q_ < NB; ++q_) { xb_[0][2 * q_] = xfrag(xt, 0, q_, c, g); xb_[0][2 * q_ + 1] = xfrag(xt, 1, q_, c, g); } \
    __builtin_amdgcn_sched_barrier(0); \
    _Pragma("unroll") for (int b_ = 0; b_ < 20 / NB; ++b_) { \
        BODY(NB * b_, xb_[b_ & 1][0], xb_[b_ & 1][1]); \
        __builtin_amdgcn_sched_barrier(0); \
        if (b_ + 1 < 20 / NB) { _Pragma("unroll") for (int q_ = 0; q_ < NB; ++q_) { xb_[(b_ + 1) & 1][2 * q_] = xfrag(xt, 0, NB * b_ + NB + q_, c, g); xb_[(b_ + 1) & 1][2 * q_ + 1] = xfrag(xt, 1, NB * b_ + NB + q_, c, g); } } \
        __builtin_amdgcn_sched_barrier(0); \
        _Pragma("unroll") for (int q_ = 1; q_ < NB; ++q_) { BODY(NB * b_ + q_, xb_[b_ & 1][2 * q_], xb_[b_ & 1][2 * q_ + 1]); } \
        __builtin_amdgcn_sched_barrier(0); \
    } } while (0)

__device__ __forceinline__ void qk_role(const WS& ws, int h, LAS unsigned char* lds) {
    const int tid = otid(), lane = tid & 63, w = __builtin_amdgcn_readfirstlane(tid >> 6);
    const int g = lane >> 4, c = lane & 15, q4 = c >> 2, p4 = c & 3;
    const rsrc_t rc = ws_rsrc(ws);
    const int mat = w >> 2, k4 = w & 3;
    bf16x8 aw[2][20];
    {
        auto stage = [&](int rd, int buf) {
#pragma unroll
            for (int i = 0; i < 8; ++i) {
                const int pc = w * 8 + i, m = pc >> 5, row = (pc & 31) * 4 + (lane >> 4), ch = (lane & 15) ^ wsw(row);
                const u16* W = m ? ws.Wk : ws.Wq;
                GLDS16(W + (size_t)(rd * 128 + row) * HD + h * DH + ch * 8, lds + buf * 65536 + pc * 1024);
            }
        };
        stage(0, 0); stage(1, 1);
#pragma unroll
        for (int rd = 0; rd < 5; ++rd) {
            if (rd > 0 && rd + 1 < 5) { asm volatile("s_waitcnt lgkmcnt(0)" ::: "memory"); __builtin_amdgcn_s_barrier(); stage(rd + 1, (rd + 1) & 1); }
            if (rd + 1 < 5) { asm volatile("s_waitcnt vmcnt(8)" ::: "memory"); } else { asm volatile("s_waitcnt vmcnt(0)" ::: "memory"); }
            __builtin_amdgcn_s_barrier();
#pragma unroll
            for (int kk = 0; kk < 4; ++kk)
#pragma unroll
                for (int i = 0; i < 2; ++i) {
                    const int r0 = kk * 32 + 8 * g + q4, r1 = r0 + 4, ec = (2 * k4 + i) * 2 + (p4 >> 1);
                    LAS unsigned char* m = lds + (rd & 1) * 65536 + mat * 32768 + (p4 & 1) * 8;
                    aw[i][rd * 4 + kk] = trfrag(m + r0 * 256 + ((ec ^ wsw(r0)) << 4), m + r1 * 256 + ((ec ^ wsw(r1)) << 4));
                }
        }
        __syncthreads();
    }
    stage_x(ws, 0, lds, w, lane);
    const unsigned obase = (mat == 0 ? O_QXF : O_KXF) + (unsigned)(h * 8 + k4) * 1024 + lane * 16;
    for (int s = 0; s < NSEG; ++s) {
        if (s < 3) { asm volatile("s_waitcnt vmcnt(0) lgkmcnt(0)" ::: "memory"); } else { asm volatile("s_waitcnt vmcnt(2) lgkmcnt(0)" ::: "memory"); }
        __builtin_amdgcn_s_barrier();
        if (tid == 0) { if (s > 0 && s < 3) ctr_add(ws, C_QK + s - 1); else if (s > 3) ctr_add(ws, C_QK + s - 2); }
        if (s + 1 < NSEG) stage_x(ws, s + 1, lds + ((s + 1) & 1) * 40960, w, lane);
        LAS unsigned char* xt = lds + (s & 1) * 40960;
        f32x4 acc[2][2];
#pragma unroll
        for (int tt = 0; tt < 2; ++tt) { acc[0][tt] = (f32x4){0, 0, 0, 0}; acc[1][tt] = (f32x4){0, 0, 0, 0}; }
#define QK_BODY(k, x0, x1) do { \
            acc[0][0] = __builtin_amdgcn_mfma_f32_16x16x32_bf16(aw[0][k], x0, acc[0][0], 0, 0, 0); acc[1][0] = __builtin_amdgcn_mfma_f32_16x16x32_bf16(aw[1][k], x0, acc[1][0], 0, 0, 0); \
            acc[0][1] = __builtin_amdgcn_mfma_f32_16x16x32_bf16(aw[0][k], x1, acc[0][1], 0, 0, 0); acc[1][1] = __builtin_amdgcn_mfma_f32_16x16x32_bf16(aw[1][k], x1, acc[1][1], 0, 0, 0); } while (0)
        XPIPE(xt, QK_BODY, 2);
#pragma unroll
        for (int tt = 0; tt < 2; ++tt) st16(rc, obase + (unsigned)s * 262144u + tt * 4096, pack8(acc[0][tt], acc[1][tt]));
    }
    asm volatile("s_waitcnt vmcnt(0)" ::: "memory");
    __syncthreads();
    if (tid == 0) { ctr_add(ws, C_QK + NSEG - 2); ctr_add(ws, C_QK + NSEG - 1); }
}

__device__ __forceinline__ void vo_role(const WS& ws, int h, LAS unsigned char* lds) {
    const int tid = otid(), lane = tid & 63, w = __builtin_amdgcn_readfirstlane(tid >> 6);
    const int g = lane >> 4, c = lane & 15, q4 = c >> 2, p4 = c & 3;
    const rsrc_t rc = ws_rsrc(ws);
    bf16x8 av[20], bo[5][4];
    {
        auto stage = [&](int rd, int buf) {
#pragma unroll
            for (int i = 0; i < 8; ++i) {
                const int pc = w * 8 + i, m = pc >> 5, row = (pc & 31) * 4 + (lane >> 4);
                if (m == 0) GLDS16(ws.Wv + (size_t)(rd * 128 + row) * HD + h * DH + ((lane & 15) ^ wsw(row)) * 8, lds + buf * 65536 + pc * 1024);
                else GLDS16(ws.Wo + (size_t)(h * DH + row) * D + rd * 128 + ((lane & 15) ^ wsw2(row)) * 8, lds + buf * 65536 + pc * 1024);
            }
        };
        stage(0, 0); stage(1, 1);
#pragma unroll
        for (int rd = 0; rd < 5; ++rd) {
            if (rd > 0 && rd + 1 < 5) { asm volatile("s_waitcnt lgkmcnt(0)" ::: "memory"); __builtin_amdgcn_s_barrier(); stage(rd + 1, (rd + 1) & 1); }
            if (rd + 1 < 5) { asm volatile("s_waitcnt vmcnt(8)" ::: "memory"); } else { asm volatile("s_waitcnt vmcnt(0)" ::: "memory"); }
            __builtin_amdgcn_s_barrier();
            LAS unsigned char* m = lds + (rd & 1) * 65536 + (p4 & 1) * 8;
            const int ec = w * 2 + (p4 >> 1);
#pragma unroll
            for (int kk = 0; kk < 4; ++kk) {
                const int r0 = kk * 32 + 8 * g + q4, r1 = r0 + 4;
                av[rd * 4 + kk] = trfrag(m + r0 * 256 + ((ec ^ wsw(r0)) << 4), m + r1 * 256 + ((ec ^ wsw(r1)) << 4));
                const int c0 = kk * 32 + 4 * g + q4, c1 = c0 + 16;
                bo[rd][kk] = trfrag(m + 32768 + c0 * 256 + ((ec ^ wsw2(c0)) << 4), m + 32768 + c1 * 256 + ((ec ^ wsw2(c1)) << 4));
            }
        }
        __syncthreads();
    }
    LAS unsigned char* vf = lds + 81920;
    stage_x(ws, 0, lds, w, lane);
    for (int s = 0; s < NSEG; ++s) {
        if (s < 3) { asm volatile("s_waitcnt vmcnt(0) lgkmcnt(0)" ::: "memory"); } else { asm volatile("s_waitcnt vmcnt(5) lgkmcnt(0)" ::: "memory"); }
        __builtin_amdgcn_s_barrier();
        if (tid == 0) { if (s > 0 && s < 3) ctr_add(ws, C_VW + s - 1); else if (s > 3) ctr_add(ws, C_VW + s - 2); }
        if (s + 1 < NSEG) stage_x(ws, s + 1, lds + ((s + 1) & 1) * 40960, w, lane);
        LAS unsigned char* xt = lds + (s & 1) * 40960;
        f32x4 a0 = {0, 0, 0, 0}, a1 = {0, 0, 0, 0};
#define VO_BODY(k, x0, x1) do { a0 = __builtin_amdgcn_mfma_f32_16x16x32_bf16(av[k], x0, a0, 0, 0, 0); a1 = __builtin_amdgcn_mfma_f32_16x16x32_bf16(av[k], x1, a1, 0, 0, 0); } while (0)
        XPIPE(xt, VO_BODY, 4);
        { v2u p0 = {pk2(a0[0], a0[1]), pk2(a0[2], a0[3])}, p1 = {pk2(a1[0], a1[1]), pk2(a1[2], a1[3])};
          *(LAS v2u*)(vf + (w >> 1) * 1024 + lane * 16 + (w & 1) * 8) = p0;
          *(LAS v2u*)(vf + (4 + (w >> 1)) * 1024 + lane * 16 + (w & 1) * 8) = p1; }
        asm volatile("s_waitcnt lgkmcnt(0)" ::: "memory");
        __builtin_amdgcn_s_barrier();
        bf16x8 v0[4], v1[4];
#pragma unroll
        for (int kk = 0; kk < 4; ++kk) { v0[kk] = __builtin_bit_cast(bf16x8, *(LAS v4u*)(vf + kk * 1024 + lane * 16)); v1[kk] = __builtin_bit_cast(bf16x8, *(LAS v4u*)(vf + (4 + kk) * 1024 + lane * 16)); }
#pragma unroll
        for (int cc = 0; cc < 5; ++cc) {
            f32x4 d0 = {0, 0, 0, 0}, d1 = {0, 0, 0, 0};
#pragma unroll
            for (int kk = 0; kk < 4; ++kk) {
                d0 = __builtin_amdgcn_mfma_f32_16x16x32_bf16(v0[kk], bo[cc][kk], d0, 0, 0, 0);
                d1 = __builtin_amdgcn_mfma_f32_16x16x32_bf16(v1[kk], bo[cc][kk], d1, 0, 0, 0);
            }
            const int j = (cc * 128 + w * 16 + c) >> 3;
            st16(rc, O_VWF + (unsigned)(((s * 80 + j) * 16 + (h >> 1)) * 64 + ((h & 1) * 8 + (c & 7) + 16 * g)) * 16u, pack8(d0, d1));
        }
    }
    asm volatile("s_waitcnt vmcnt(0)" ::: "memory");
    __syncthreads();
    if (tid == 0) { ctr_add(ws, C_VW + NSEG - 2); ctr_add(ws, C_VW + NSEG - 1); }
}

__device__ __forceinline__ void task_wvo2(const WS& ws, int h, int cc, LAS unsigned char* lds) {
    const int tid = otid(), lane = tid & 63, w = __builtin_amdgcn_readfirstlane(tid >> 6);
    const int g = lane >> 4, c = lane & 15, q4 = c >> 2, p4 = c & 3;
    const rsrc_t rw = ws_rsrc(ws);
#pragma unroll
    for (int i = 0; i < 4; ++i) {
        const int pc = w * 4 + i, row = pc * 4 + (lane >> 4), ch = (lane & 15) ^ wsw(row);
        GLDS16(ws.Wo + (size_t)(h * DH + row) * D + cc * 128 + ch * 8, lds + pc * 1024);
    }
    v4u a[3][2][4];
#pragma unroll
    for (int i = 0; i < 3; ++i) {
        const int u = (w + 8 * i < 20) ? w + 8 * i : w, ks = u >> 1, hf = u & 1;
#pragma unroll
        for (int r2 = 0; r2 < 2; ++r2) {
            const int dd = dmap(ks, c >> 2, 4 * (2 * hf + r2) + (c & 3));
            const v4u* src = (const v4u*)(ws.Wv + (size_t)dd * HD + h * DH + 8 * g);
#pragma unroll
            for (int kk = 0; kk < 4; ++kk) a[i][r2][kk] = src[kk * 4];
        }
    }
    asm volatile("s_waitcnt vmcnt(24)" ::: "memory");
    __builtin_amdgcn_s_barrier();
#pragma unroll
    for (int half = 0; half < 2; ++half) {
        bf16x8 bw[4][4];
#pragma unroll
        for (int ct = 0; ct < 4; ++ct)
#pragma unroll
            for (int kk = 0; kk < 4; ++kk) { const int r0 = kk * 32 + 8 * g + q4, r1 = r0 + 4, ec = (half * 4 + ct) * 2 + (p4 >> 1); LAS unsigned char* m = lds + (p4 & 1) * 8;
                bw[ct][kk] = trfrag(m + r0 * 256 + ((ec ^ wsw(r0)) << 4), m + r1 * 256 + ((ec ^ wsw(r1)) << 4)); }
#pragma unroll
        for (int i = 0; i < 3; ++i) {
            const int u = w + 8 * i, ks = u >> 1, hf = u & 1;
            if (u < 20) {
#pragma unroll
                for (int ct = 0; ct < 4; ++ct) {
                    f32x4 d0 = {0, 0, 0, 0}, d1 = {0, 0, 0, 0};
#pragma unroll
                    for (int kk = 0; kk < 4; ++kk) {
                        d0 = __builtin_amdgcn_mfma_f32_16x16x32_bf16(__builtin_bit_cast(bf16x8, a[i][0][kk]), bw[ct][kk], d0, 0, 0, 0);
                        d1 = __builtin_amdgcn_mfma_f32_16x16x32_bf16(__builtin_bit_cast(bf16x8, a[i][1][kk]), bw[ct][kk], d1, 0, 0, 0);
                    }
                    const int j = (cc * 128 + (half * 4 + ct) * 16 + c) >> 3;
                    st16(rw, O_WVOF + (unsigned)((((j * 16 + (h >> 1)) * 10 + ks) * 2 + hf) * 64 + ((h & 1) * 8 + (c & 7) + 16 * g)) * 16u, pack8(d0, d1));
                }
            }
        }
    }
}

__device__ __forceinline__ void task_f1s(const WS& ws, int cb, int s0, int ns, LAS unsigned char* lds) {
    const int tid = otid(), lane = tid & 63, w = __builtin_amdgcn_readfirstlane(tid >> 6);
    const int g = lane >> 4, c = lane & 15;
    const rsrc_t rw = ws_rsrc(ws);
    stage_x(ws, s0, lds, w, lane);
    v4u bh[20], bl[20];
    const unsigned fo = O_FF + (unsigned)(cb * 8 + w) * 40960 + lane * 16;
#pragma unroll
    for (int k = 0; k < 20; ++k) { bh[k] = ld16(rw, fo + k * 2048); bl[k] = ld16(rw, fo + k * 2048 + 1024); }
    const int col = cb * 128 + w * 16 + c;
    for (int i = 0; i < ns; ++i) {
        const int s = s0 + i;
        if (i == 0) { asm volatile("s_waitcnt vmcnt(0) lgkmcnt(0)" ::: "memory"); } else { asm volatile("s_waitcnt vmcnt(2) lgkmcnt(0)" ::: "memory"); }
        __builtin_amdgcn_s_barrier();
        if (i + 1 < ns) stage_x(ws, s + 1, lds + ((i + 1) & 1) * 40960, w, lane);
        LAS unsigned char* xt = lds + (i & 1) * 40960;
        f32x4 a0 = {0, 0, 0, 0}, a1 = {0, 0, 0, 0};
#define F1_BODY(k, x0, x1) do { \
            a0 = __builtin_amdgcn_mfma_f32_16x16x32_bf16(x0, __builtin_bit_cast(bf16x8, bl[k]), a0, 0, 0, 0); a1 = __builtin_amdgcn_mfma_f32_16x16x32_bf16(x1, __builtin_bit_cast(bf16x8, bl[k]), a1, 0, 0, 0); \
            a0 = __builtin_amdgcn_mfma_f32_16x16x32_bf16(x0, __builtin_bit_cast(bf16x8, bh[k]), a0, 0, 0, 0); a1 = __builtin_amdgcn_mfma_f32_16x16x32_bf16(x1, __builtin_bit_cast(bf16x8, bh[k]), a1, 0, 0, 0); } while (0)
        XPIPE(xt, F1_BODY, 2);
        st16(rw, O_XT + (unsigned)(col * 2048 + s * 32 + 4 * g) * 4u, __builtin_bit_cast(v4u, a0));
        st16(rw, O_XT + (unsigned)(col * 2048 + s * 32 + 16 + 4 * g) * 4u, __builtin_bit_cast(v4u, a1));
    }
}

__device__ __forceinline__ void task_f3s(const WS& ws, int db, int s0, int ns, LAS unsigned char* lds) {
    const int tid = otid(), lane = tid & 63, w = __builtin_amdgcn_readfirstlane(tid >> 6);
    const int g = lane >> 4, c = lane & 15, q4 = c >> 2, p4 = c & 3;
    const rsrc_t rw = ws_rsrc(ws);
    v4u gh[20], gl[20];
    const unsigned go = O_GF + (unsigned)(db * 8 + w) * 40960 + lane * 16;
#pragma unroll
    for (int k = 0; k < 20; ++k) { gh[k] = ld16(rw, go + k * 2048); gl[k] = ld16(rw, go + k * 2048 + 1024); }
    v4u st[5];
    const int pl = tid >> 8, t8 = tid & 255;
    const unsigned gsrc = (pl ? O_YL : O_YH) + t8 * 16;
    LAS unsigned char* ldst = lds + pl * 20480 + (t8 >> 2) * 64 + (((t8 & 3) ^ (((t8 >> 5) & 1) << 1)) << 4);
    auto gload = [&](int s, int hf) {
#pragma unroll
        for (int i = 0; i < 5; ++i) st[i] = ld16(rw, gsrc + (unsigned)s * 40960u + hf * 20480 + i * 4096);
    };
    auto lwrite = [&](int buf) {
#pragma unroll
        for (int i = 0; i < 5; ++i) *(LAS v4u*)(ldst + buf * 40960 + i * 4096) = st[i];
    };
    gload(s0, 0); lwrite(0); gload(s0, 1);
    __syncthreads();
    const float sc = 1.0f / (2048.0f * 640.0f);
    for (int i = 0; i < ns; ++i) {
        const int s = s0 + i;
        f32x4 a0 = {0, 0, 0, 0}, a1 = {0, 0, 0, 0};
#pragma unroll
        for (int hf = 0; hf < 2; ++hf) {
            LAS unsigned char* base = lds + hf * 40960;
#pragma unroll
            for (int kk = 0; kk < 10; ++kk) {
                const int k = hf * 10 + kk;
                const int r0 = kk * 32 + 8 * g + q4, r1 = r0 + 4, sw = (g & 1) << 5;
                LAS unsigned char* m0 = base + r0 * 64 + p4 * 8; LAS unsigned char* m1 = base + r1 * 64 + p4 * 8;
                bf16x8 yh0 = trfrag(m0 + (0 ^ sw), m1 + (0 ^ sw)), yh1 = trfrag(m0 + (32 ^ sw), m1 + (32 ^ sw));
                bf16x8 yl0 = trfrag(m0 + 20480 + (0 ^ sw), m1 + 20480 + (0 ^ sw)), yl1 = trfrag(m0 + 20480 + (32 ^ sw), m1 + 20480 + (32 ^ sw));
                a0 = __builtin_amdgcn_mfma_f32_16x16x32_bf16(__builtin_bit_cast(bf16x8, gl[k]), yh0, a0, 0, 0, 0);
                a1 = __builtin_amdgcn_mfma_f32_16x16x32_bf16(__builtin_bit_cast(bf16x8, gl[k]), yh1, a1, 0, 0, 0);
                a0 = __builtin_amdgcn_mfma_f32_16x16x32_bf16(__builtin_bit_cast(bf16x8, gh[k]), yl0, a0, 0, 0, 0);
                a1 = __builtin_amdgcn_mfma_f32_16x16x32_bf16(__builtin_bit_cast(bf16x8, gh[k]), yl1, a1, 0, 0, 0);
                a0 = __builtin_amdgcn_mfma_f32_16x16x32_bf16(__builtin_bit_cast(bf16x8, gh[k]), yh0, a0, 0, 0, 0);
                a1 = __builtin_amdgcn_mfma_f32_16x16x32_bf16(__builtin_bit_cast(bf16x8, gh[k]), yh1, a1, 0, 0, 0);
                if (kk & 1) __builtin_amdgcn_sched_barrier(0);
            }
            if (hf == 0) { lwrite(1); if (i + 1 < ns) gload(s + 1, 0); }
            else if (i + 1 < ns) { lwrite(0); gload(s + 1, 1); }
            __syncthreads();
        }
        const int d0 = db * 128 + w * 16 + 4 * g;
        v2u o0 = {pk2(a0[0] * sc, a0[1] * sc), pk2(a0[2] * sc, a0[3] * sc)}, o1 = {pk2(a1[0] * sc, a1[1] * sc), pk2(a1[2] * sc, a1[3] * sc)};
        __builtin_amdgcn_raw_buffer_store_b64(o0, rw, O_ABUF + (unsigned)((s * 32 + c) * D + d0) * 2u, 0, 16);
        __builtin_amdgcn_raw_buffer_store_b64(o1, rw, O_ABUF + (unsigned)((s * 32 + 16 + c) * D + d0) * 2u, 0, 16);
    }
}

__device__ __forceinline__ void task_wt2(const WS& ws, int tb, LAS unsigned char* lds) {
    const int tid = otid(), lane = tid & 63, w = __builtin_amdgcn_readfirstlane(tid >> 6);
    const rsrc_t rw = ws_rsrc(ws);
    LAS unsigned* tile = (LAS unsigned*)lds;
#pragma unroll
    for (int i = 0; i < 8; ++i) {
        const int e = w * 8 + i, tl = e >> 4, hf = (e >> 3) & 1, k2 = e & 7;
        const int p = (tb * 4 + tl) + 128 * hf, k0 = p >> 4, k1 = p & 15, u = k0 + 16 * k1 + 256 * k2;
#pragma unroll
        for (int q = 0; q < 6; ++q) {
            const int f = q * 64 + lane;
            if (f < DF) tile[f * 68 + e] = (unsigned)ws.wr[u * DF + f] | ((unsigned)ws.wi[u * DF + f] << 16);
        }
    }
    __syncthreads();
    for (int q = tid; q < DF * 16; q += 512) {
        const int f = q >> 4, ch = q & 15;
        st16(rw, O_WT + (unsigned)f * 8192 + tb * 256 + ch * 16, *(LAS v4u*)(tile + f * 68 + ch * 4));
    }
    __syncthreads();
}

__device__ __forceinline__ unsigned xcc_id() { return (unsigned)__builtin_amdgcn_s_getreg(20 | (3 << 11)) & 0xfu; }

constexpr int F1Q = 16, F3Q = 16;
constexpr int G_WVO = 0, G_DFT = 0, G_WT = G_DFT + 50, G_F1 = G_WT + 32, G_F2 = G_F1 + 5 * F1Q, G_F3 = G_F2 + 81, G_ATT = G_F3 + 5 * F3Q;
constexpr int ATT_PER = 10;
constexpr int G_END = G_ATT + NSEG * ATT_PER;
constexpr int NSTATIC = 82;

__device__ __forceinline__ void signal(rsrc_t rc, const WS& ws, unsigned ctr) {
    asm volatile("s_waitcnt vmcnt(0)" ::: "memory");
    __syncthreads();
    if (threadIdx.x == 0) ctr_add(ws, ctr);
}

__device__ __forceinline__ void worker(const WS& ws, int first, LAS unsigned char* lds) {
    const int tid = threadIdx.x;
    const rsrc_t rc = ws_rsrc(ws);
    LAS int* slot = (LAS int*)(lds + LDS_BYTES - 64);
    bool table = false;
    for (int iter = 0;; ++iter) {
        int t;
        if (iter == 0 && first >= 0) t = first;
        else {
            if (tid == 0) { int q = NSTATIC + (int)ctr_add(ws, C_GHEAD); *slot = q < G_END ? q : -1; }
            __syncthreads();
            t = __builtin_amdgcn_readfirstlane(*slot);
            __syncthreads();
        }
        if (t < 0) break;
        if (t < G_WT) { for (int i = 0; i < 4; ++i) wave_dftmat(ws, ((t - G_DFT) * 4 + i) * 8 + (tid >> 6)); signal(rc, ws, C_DFT); }
        else if (t < G_F1) { task_wt2(ws, t - G_WT, lds); signal(rc, ws, C_WT); table = false; }
        else if (t < G_F2) {
            const int q = t - G_F1;
            wait_ctr(rc, C_DFT, 50);
            task_f1s(ws, q % 5, (q / 5) * (64 / F1Q), 64 / F1Q, lds); signal(rc, ws, C_F1); table = false;
        } else if (t < G_F3) {
            wait_ctr(rc, C_F1, 5 * F1Q); wait_ctr(rc, C_WT, 32);
            task_f2(ws, t - G_F2, lds, !table); table = true; signal(rc, ws, C_F2);
        } else if (t < G_ATT) {
            const int q = t - G_F3;
            wait_ctr(rc, C_F2, 81);
            task_f3s(ws, q % 5, (q / 5) * (64 / F3Q), 64 / F3Q, lds); signal(rc, ws, C_F3 + q / 5); table = false;
        } else {
            const int q = t - G_ATT, s = q / ATT_PER, r = q % ATT_PER;
            wait_ctr(rc, C_ATTA + s, 64); wait_ctr(rc, C_F3 + s / (64 / F3Q), 5); wait_ctr(rc, C_VW + s, 32); wait_prog(rc, s + 2);
            task_attB(ws, s, r, lds); table = false;
        }
    }
}

__global__ void __launch_bounds__(512, 2) k_mega(WS ws, ChainCfg cfg) {
    extern __shared__ __attribute__((aligned(16))) unsigned char smem[];
    LAS unsigned char* lds = (LAS unsigned char*)smem;
    LAS int* slot = (LAS int*)(lds + LDS_BYTES - 64);
    const rsrc_t rc = ws_rsrc(ws);
    if (threadIdx.x == 0) {
        const unsigned x = xcc_id() & 7u;
        const unsigned t = ctr_add(ws, C_XCD + x);
        int sl = (int)(x * 32 + t);
        if (t >= 32) {
            const unsigned k = ctr_add(ws, C_OVF);
            unsigned spins = 0;
            for (;;) {
                unsigned tot = 0; for (int i = 0; i < 8; ++i) tot += ctr_get(rc, C_XCD + i);
                if (tot >= gridDim.x) break;
                __builtin_amdgcn_s_sleep(8); asm volatile("" ::: "memory");
                if (++spins > (1u << 22)) { set_err(rc, 9); break; }
            }
            unsigned acc = 0; sl = -1;
            for (int i = 0; i < 8; ++i) { const unsigned n = ctr_get(rc, C_XCD + i); const unsigned fr = n < 32 ? 32 - n : 0; if (sl < 0 && k < acc + fr) sl = i * 32 + (int)(n + (k - acc)); acc += fr; }
        }
        *slot = sl;
    }
    __syncthreads();
    const int sl = __builtin_amdgcn_readfirstlane(*slot);
    __syncthreads();
    if (sl < 0) return;
    const int x = sl >> 5, t = sl & 31;
    int first = -1;
    if (x < 4) {
        if (t < 8) { head_role(ws, cfg, x * 8 + t, lds); return; }
        if (t < 16) { u_role(ws, cfg, x * 8 + (t - 8), lds); return; }
        if (t < 26) { st2_role(ws, cfg, x * 10 + (t - 16), lds); return; }
        if (sl == 31) { sched_role(ws, cfg); return; }
        first = 64 + x * 6 + (t - 26) - (x > 0 ? 1 : 0);
    } else {
        if (t < 8) qk_role(ws, (x - 4) * 8 + t, lds);
        else if (t < 16) vo_role(ws, (x - 4) * 8 + (t - 8), lds);
        else first = (x - 4) * 16 + (t - 16);
    }
    if (first >= NSTATIC) first = -1;
    worker(ws, first, lds);
}

extern "C" void kernel_launch(void* const* d_in, const int* in_sizes, int n_in, void* d_out, int out_size, void* d_ws, size_t ws_size, hipStream_t stream) {
    WS ws;
    ws.x = (const u16*)d_in[0]; ws.wr = (const u16*)d_in[1]; ws.wi = (const u16*)d_in[2]; ws.mem0 = (const u16*)d_in[4];
    ws.Wq = (const u16*)d_in[5]; ws.Wk = (const u16*)d_in[6]; ws.Wv = (const u16*)d_in[7]; ws.Wo = (const u16*)d_in[8]; ws.gw = (const u16*)d_in[10];
    ws.out = (u16*)d_out; ws.wsb = (unsigned char*)d_ws;
    (void)hipFuncSetAttribute((const void*)k_mega, hipFuncAttributeMaxDynamicSharedMemorySize, LDS_BYTES);
    (void)hipMemsetAsync(d_ws, 0xFF, CTL_END, stream);
#define CFG_T1 98
#define CFG_T2 80
#define CFG_T3 148
#define CFG_PAD 120
#define CFG_D0 -8
#define CFG_PEN 35
#define CFG_SD1 140
#define CFG_SD2 60
    ChainCfg cfg{CFG_T1, CFG_T2, CFG_T3, CFG_PAD, CFG_D0, CFG_PEN, CFG_SD1, CFG_SD2};
    k_mega<<<256, 512, LDS_BYTES, stream>>>(ws, cfg);
}
```

```cpp
constexpr unsigned O_FF = O_FFT;
constexpr unsigned O_GF = O_FF + 40 * 20 * 2 * 1024;
constexpr unsigned O_WT = O_GF + 40 * 20 * 2 * 1024;
constexpr unsigned O_XT = O_WT + 324 * 2048 * 4;
constexpr unsigned O_YH = O_XT + 640 * 2048 * 4;
constexpr unsigned O_YL = O_YH + 640 * 2048 * 2;
constexpr unsigned O_FFT_END = O_YL + 640 * 2048 * 2;

#define split2(a, b, hi, lo) do { unsigned h_ = pk2(a, b); hi = h_; lo = pk2((a) - l16f(h_), (b) - h16f(h_)); } while (0)

__device__ __forceinline__ void wave_dftmat(const WS& ws, int item) {
    const int lane = otid() & 63, g = lane >> 4, c = lane & 15;
    const int which = item / 800, tile = (item % 800) / 20, k = item % 20;
    const rsrc_t rw = ws_rsrc(ws);
    float v[8];
#pragma unroll
    for (int j = 0; j < 8; ++j) {
        int n, d;
        if (which == 0) { n = 16 * tile + c; d = 32 * k + 8 * g + j; }
        else { n = 32 * k + 8 * g + j; d = 16 * tile + c; }
        const bool im = n > 320; const int f = im ? n - 320 : n;
        const int idx = (f * d) % D;
        float sn, cs; sincospif((float)idx * (2.0f / D), &sn, &cs);
        float val = im ? -sn : cs;
        if (which == 1 && f != 0 && f != 320) val *= 2.0f;
        v[j] = val;
    }
    v4u hi, lo;
    split2(v[0], v[1], hi.x, lo.x); split2(v[2], v[3], hi.y, lo.y); split2(v[4], v[5], hi.z, lo.z); split2(v[6], v[7], hi.w, lo.w);
    const unsigned o = (which ? O_GF : O_FF) + (unsigned)((tile * 20 + k) * 2) * 1024 + lane * 16;
    st16(rw, o, hi); st16(rw, o + 1024, lo);
}

__device__ __forceinline__ void task_wt(const WS& ws, int fb, LAS unsigned char* lds) {
    const int tid = otid();
    const rsrc_t rw = ws_rsrc(ws);
    LAS unsigned* tile = (LAS unsigned*)lds;
    const int f0 = fb * 16;
    for (int q = tid; q < 2048 * 16; q += 512) {
        const int u = q >> 4, fl = q & 15, f = f0 + fl;
        unsigned v = 0;
        if (f < DF) v = (unsigned)ws.wr[u * DF + f] | ((unsigned)ws.wi[u * DF + f] << 16);
        const int k0 = u & 15, k1 = (u >> 4) & 15, k2 = u >> 8, p = k0 * 16 + k1, t = p & 127, half = p >> 7;
        tile[fl * 2052 + t * 16 + half * 8 + k2] = v;
    }
    __syncthreads();
    for (int q = tid; q < 16 * 512; q += 512) {
        const int fl = q >> 9, ch = q & 511;
        if (f0 + fl < 324) st16(rw, O_WT + (unsigned)(f0 + fl) * 8192 + ch * 16, *(LAS v4u*)(tile + fl * 2052 + ch * 4));
    }
    __syncthreads();
}

__device__ __forceinline__ void task_f1(const WS& ws, int b, int cb) {
    const int tid = otid(), lane = tid & 63, w = __builtin_amdgcn_readfirstlane(tid >> 6);
    const int g = lane >> 4, c = lane & 15;
    const rsrc_t rw = ws_rsrc(ws);
    const int th = w & 1, cg = w >> 1;
    f32x4 acc[4][2];
#pragma unroll
    for (int t = 0; t < 4; ++t) { acc[t][0] = (f32x4){0, 0, 0, 0}; acc[t][1] = (f32x4){0, 0, 0, 0}; }
    const u16* xa = ws.x + (size_t)(b * 128 + th * 64 + c) * D + 8 * g;
    const unsigned fo = O_FF + (unsigned)(cb * 8 + cg * 2) * 40960 + lane * 16;
#pragma unroll 2
    for (int k = 0; k < 20; ++k) {
        v4u a[4], bh[2], bl[2];
#pragma unroll
        for (int t = 0; t < 4; ++t) a[t] = *(const v4u*)(xa + (size_t)t * 16 * D + k * 32);
#pragma unroll
        for (int n = 0; n < 2; ++n) { bh[n] = ld16(rw, fo + n * 40960 + k * 2048); bl[n] = ld16(rw, fo + n * 40960 + k * 2048 + 1024); }
#pragma unroll
        for (int t = 0; t < 4; ++t)
#pragma unroll
            for (int n = 0; n < 2; ++n) {
                acc[t][n] = __builtin_amdgcn_mfma_f32_16x16x32_bf16(__builtin_bit_cast(bf16x8, a[t]), __builtin_bit_cast(bf16x8, bh[n]), acc[t][n], 0, 0, 0);
                acc[t][n] = __builtin_amdgcn_mfma_f32_16x16x32_bf16(__builtin_bit_cast(bf16x8, a[t]), __builtin_bit_cast(bf16x8, bl[n]), acc[t][n], 0, 0, 0);
            }
    }
#pragma unroll
    for (int t = 0; t < 4; ++t)
#pragma unroll
        for (int n = 0; n < 2; ++n) {
            const int col = cb * 128 + (cg * 2 + n) * 16 + c, s0 = b * 128 + th * 64 + t * 16 + 4 * g;
            st16(rw, O_XT + (unsigned)(col * 2048 + s0) * 4u, __builtin_bit_cast(v4u, acc[t][n]));
        }
}

struct cf { float x, y; };
__device__ __forceinline__ cf cadd(cf a, cf b) { return {a.x + b.x, a.y + b.y}; }
__device__ __forceinline__ cf csub(cf a, cf b) { return {a.x - b.x, a.y - b.y}; }
__device__ __forceinline__ cf cmul(cf a, cf b) { return {a.x * b.x - a.y * b.y, a.x * b.y + a.y * b.x}; }
template <int DIR> __device__ __forceinline__ cf mulI(cf a) { return DIR < 0 ? cf{a.y, -a.x} : cf{-a.y, a.x}; }
template <int DIR> __device__ __forceinline__ void dft4(cf& a, cf& b, cf& c, cf& d) {
    cf s0 = cadd(a, c), s1 = csub(a, c), s2 = cadd(b, d), s3 = mulI<DIR>(csub(b, d));
    a = cadd(s0, s2); c = csub(s0, s2); b = cadd(s1, s3); d = csub(s1, s3);
}
template <int DIR> __device__ __forceinline__ cf tw16(cf a, int k) {
    const float C1 = 0.92387953251128674f, S1 = 0.38268343236508977f, R = 0.70710678118654752f;
    const float cs[10] = {1.f, C1, R, S1, 0.f, -S1, -R, -C1, -1.f, -C1}, sn[10] = {0.f, S1, R, C1, 1.f, C1, R, S1, 0.f, -S1};
    if (k == 0) return a;
    const float c = cs[k], s = DIR * sn[k];
    return {a.x * c - a.y * s, a.x * s + a.y * c};
}
template <int DIR> __device__ __forceinline__ void dft16(cf (&v)[16]) {
#pragma unroll
    for (int i = 0; i < 4; ++i) dft4<DIR>(v[i], v[i + 4], v[i + 8], v[i + 12]);
#pragma unroll
    for (int i = 1; i < 4; ++i)
#pragma unroll
        for (int k1 = 1; k1 < 4; ++k1) v[i + 4 * k1] = tw16<DIR>(v[i + 4 * k1], i * k1);
#pragma unroll
    for (int k1 = 0; k1 < 4; ++k1) dft4<DIR>(v[4 * k1], v[4 * k1 + 1], v[4 * k1 + 2], v[4 * k1 + 3]);
#pragma unroll
    for (int a = 0; a < 4; ++a)
#pragma unroll
        for (int b = a + 1; b < 4; ++b) { cf t = v[4 * a + b]; v[4 * a + b] = v[4 * b + a]; v[4 * b + a] = t; }
}
template <int DIR> __device__ __forceinline__ void dft8(cf* v) {
    dft4<DIR>(v[0], v[2], v[4], v[6]); dft4<DIR>(v[1], v[3], v[5], v[7]);
    v[3] = tw16<DIR>(v[3], 2); v[5] = tw16<DIR>(v[5], 4); v[7] = tw16<DIR>(v[7], 6);
    cf o[8];
#pragma unroll
    for (int k1 = 0; k1 < 4; ++k1) { o[k1] = cadd(v[2 * k1], v[2 * k1 + 1]); o[k1 + 4] = csub(v[2 * k1], v[2 * k1 + 1]); }
#pragma unroll
    for (int i = 0; i < 8; ++i) v[i] = o[i];
}
__device__ __forceinline__ int fpad(int i) { return i + (i >> 4); }
__device__ __forceinline__ void task_f2(const WS& ws, int fg, LAS unsigned char* lds, bool make_table) {
    const int tid = otid(), col = tid >> 7, t = tid & 127;
    const rsrc_t rw = ws_rsrc(ws);
    LAS f32x2v* tw = (LAS f32x2v*)lds;
    LAS f32x2v* buf = (LAS f32x2v*)(lds + 16384) + col * 2176;
    if (make_table) {
        for (int i = tid; i < 2048; i += 512) { float sn, cs; sincospif((float)i * (1.0f / 1024.0f), &sn, &cs); tw[i] = (f32x2v){cs, sn}; }
    }
    const int f = fg * 4 + col;
    const bool valid = f < DF, hasim = valid && f != 0 && f != 320;
    const int fr = valid ? f : 0;
    cf v[16];
#pragma unroll
    for (int s2 = 0; s2 < 16; ++s2) {
        v[s2].x = __uint_as_float(__builtin_amdgcn_raw_buffer_load_b32(rw, O_XT + (unsigned)(fr * 2048 + s2 * 128 + t) * 4u, 0, 16));
        v[s2].y = hasim ? __uint_as_float(__builtin_amdgcn_raw_buffer_load_b32(rw, O_XT + (unsigned)((320 + fr) * 2048 + s2 * 128 + t) * 4u, 0, 16)) : 0.f;
    }
    v4u wv[4];
#pragma unroll
    for (int i = 0; i < 4; ++i) wv[i] = ld16(rw, O_WT + (unsigned)fr * 8192 + t * 64 + i * 16);
    __syncthreads();
    dft16<-1>(v);
#pragma unroll
    for (int k0 = 1; k0 < 16; ++k0) { f32x2v w = tw[k0 * t]; v[k0] = cmul(v[k0], cf{w.x, -w.y}); }
#pragma unroll
    for (int k0 = 0; k0 < 16; ++k0) buf[fpad(k0 * 128 + t)] = (f32x2v){v[k0].x, v[k0].y};
    __syncthreads();
    { const int k0 = t >> 3, s0 = t & 7;
#pragma unroll
      for (int s1 = 0; s1 < 16; ++s1) { f32x2v q = buf[fpad(k0 * 128 + s1 * 8 + s0)]; v[s1] = cf{q.x, q.y}; }
      dft16<-1>(v);
#pragma unroll
      for (int k1 = 1; k1 < 16; ++k1) { f32x2v w = tw[k1 * s0 * 16]; v[k1] = cmul(v[k1], cf{w.x, -w.y}); }
      __syncthreads();
#pragma unroll
      for (int k1 = 0; k1 < 16; ++k1) buf[fpad((k0 * 16 + k1) * 8 + s0)] = (f32x2v){v[k1].x, v[k1].y};
    }
    __syncthreads();
#pragma unroll
    for (int hf = 0; hf < 2; ++hf) {
#pragma unroll
        for (int s0 = 0; s0 < 8; ++s0) { f32x2v q = buf[fpad((t + 128 * hf) * 8 + s0)]; v[hf * 8 + s0] = cf{q.x, q.y}; }
        dft8<-1>(v + hf * 8);
#pragma unroll
        for (int k2 = 0; k2 < 8; ++k2) { const unsigned ww = wv[hf * 2 + (k2 >> 2)][k2 & 3]; v[hf * 8 + k2] = cmul(v[hf * 8 + k2], cf{l16f(ww), h16f(ww)}); }
        dft8<1>(v + hf * 8);
#pragma unroll
        for (int s0 = 0; s0 < 8; ++s0) buf[fpad((t + 128 * hf) * 8 + s0)] = (f32x2v){v[hf * 8 + s0].x, v[hf * 8 + s0].y};
    }
    __syncthreads();
    { const int k0 = t >> 3, s0 = t & 7;
#pragma unroll
      for (int k1 = 0; k1 < 16; ++k1) { f32x2v q = buf[fpad((k0 * 16 + k1) * 8 + s0)]; v[k1] = cf{q.x, q.y}; }
#pragma unroll
      for (int k1 = 1; k1 < 16; ++k1) { f32x2v w = tw[k1 * s0 * 16]; v[k1] = cmul(v[k1], cf{w.x, w.y}); }
      dft16<1>(v);
      __syncthreads();
#pragma unroll
      for (int s1 = 0; s1 < 16; ++s1) buf[fpad(k0 * 128 + s1 * 8 + s0)] = (f32x2v){v[s1].x, v[s1].y};
    }
    __syncthreads();
#pragma unroll
    for (int k0 = 0; k0 < 16; ++k0) { f32x2v q = buf[fpad(k0 * 128 + t)]; v[k0] = cf{q.x, q.y}; }
#pragma unroll
    for (int k0 = 1; k0 < 16; ++k0) { f32x2v w = tw[k0 * t]; v[k0] = cmul(v[k0], cf{w.x, w.y}); }
    dft16<1>(v);
    __syncthreads();
#pragma unroll
    for (int s2 = 0; s2 < 16; ++s2) buf[fpad(s2 * 128 + t)] = (f32x2v){v[s2].x, v[s2].y};
    __syncthreads();
#pragma unroll
    for (int hf = 0; hf < 2; ++hf) {
        const int s0 = 8 * (t + 128 * hf);
        f32x2v q[8];
#pragma unroll
        for (int i = 0; i < 8; ++i) q[i] = buf[fpad(s0 + i)];
        v4u rh, rl, ih, il;
        split2(q[0].x, q[1].x, rh.x, rl.x); split2(q[2].x, q[3].x, rh.y, rl.y); split2(q[4].x, q[5].x, rh.z, rl.z); split2(q[6].x, q[7].x, rh.w, rl.w);
        split2(q[0].y, q[1].y, ih.x, il.x); split2(q[2].y, q[3].y, ih.y, il.y); split2(q[4].y, q[5].y, ih.z, il.z); split2(q[6].y, q[7].y, ih.w, il.w);
        const unsigned yo = (unsigned)(((s0 >> 5) * 640 + f) * 32 + (s0 & 31)) * 2u;
        if (valid) { st16(rw, O_YH + yo, rh); st16(rw, O_YL + yo, rl); }
        if (hasim) { st16(rw, O_YH + yo + 320 * 64, ih); st16(rw, O_YL + yo + 320 * 64, il); }
    }
    __syncthreads();
}

__device__ __forceinline__ void stage_x(const WS& ws, int s, LAS unsigned char* dst, int w, int lane) {
#pragma unroll
    for (int i = 0; i < 5; ++i) {
        const int pc = w * 5 + i, q = pc * 64 + lane, row = q / 80, slot = q - row * 80;
        glds16_asm(ws.x + (size_t)(s * 32 + row) * D + (slot ^ (row & 15)) * 8, dst + pc * 1024);
    }
}
__device__ __forceinline__ bf16x8 xfrag(LAS unsigned char* xt, int tt, int k, int c, int g) {
    return __builtin_bit_cast(bf16x8, *(LAS v4u*)(xt + (c * 1280 + (((4 * (k & 3) + g) ^ c) << 4)) + (tt * 20480 + (k >> 2) * 256)));
}

#define XPIPE(xt, BODY, NB) do { \
    bf16x8 xb_[2][2 * NB]; \
    _Pragma("unroll") for (int q_ = 0; q_ < NB; ++q_) { xb_[0][2 * q_] = xfrag(xt, 0, q_, c, g); xb_[0][2 * q_ + 1] = xfrag(xt, 1, q_, c, g); } \
    __builtin_amdgcn_sched_barrier(0); \
    _Pragma("unroll") for (int b_ = 0; b_ < 20 / NB; ++b_) { \
        BODY(NB * b_, xb_[b_ & 1][0], xb_[b_ & 1][1]); \
        __builtin_amdgcn_sched_barrier(0); \
        if (b_ + 1 < 20 / NB) { _Pragma("unroll") for (int q_ = 0; q_ < NB; ++q_) { xb_[(b_ + 1) & 1][2 * q_] = xfrag(xt, 0, NB * b_ + NB + q_, c, g); xb_[(b_ + 1) & 1][2 * q_ + 1] = xfrag(xt, 1, NB * b_ + NB + q_, c, g); } } \
        __builtin_amdgcn_sched_barrier(0); \
        _Pragma("unroll") for (int q_ = 1; q_ < NB; ++q_) { BODY(NB * b_ + q_, xb_[b_ & 1][2 * q_], xb_[b_ & 1][2 * q_ + 1]); } \
        __builtin_amdgcn_sched_barrier(0); \
    } } while (0)

__device__ __forceinline__ void qk_role(const WS& ws, int h, LAS unsigned char* lds) {
    const int tid = otid(), lane = tid & 63, w = __builtin_amdgcn_readfirstlane(tid >> 6);
    const int g = lane >> 4, c = lane & 15, q4 = c >> 2, p4 = c & 3;
    const rsrc_t rc = ws_rsrc(ws);
    const int mat = w >> 2, k4 = w & 3;
    bf16x8 aw[2][20];
    {
        auto stage = [&](int rd, int buf) {
#pragma unroll
            for (int i = 0; i < 8; ++i) {
                const int pc = w * 8 + i, m = pc >> 5, row = (pc & 31) * 4 + (lane >> 4), ch = (lane & 15) ^ wsw(row);
                const u16* W = m ? ws.Wk : ws.Wq;
                GLDS16(W + (size_t)(rd * 128 + row) * HD + h * DH + ch * 8, lds + buf * 65536 + pc * 1024);
            }
        };
        stage(0, 0); stage(1, 1);
#pragma unroll
        for (int rd = 0; rd < 5; ++rd) {
            if (rd > 0 && rd + 1 < 5) { asm volatile("s_waitcnt lgkmcnt(0)" ::: "memory"); __builtin_amdgcn_s_barrier(); stage(rd + 1, (rd + 1) & 1); }
            if (rd + 1 < 5) { asm volatile("s_waitcnt vmcnt(8)" ::: "memory"); } else { asm volatile("s_waitcnt vmcnt(0)" ::: "memory"); }
            __builtin_amdgcn_s_barrier();
#pragma unroll
            for (int kk = 0; kk < 4; ++kk)
#pragma unroll
                for (int i = 0; i < 2; ++i) {
                    const int r0 = kk * 32 + 8 * g + q4, r1 = r0 + 4, ec = (2 * k4 + i) * 2 + (p4 >> 1);
                    LAS unsigned char* m = lds + (rd & 1) * 65536 + mat * 32768 + (p4 & 1) * 8;
                    aw[i][rd * 4 + kk] = trfrag(m + r0 * 256 + ((ec ^ wsw(r0)) << 4), m + r1 * 256 + ((ec ^ wsw(r1)) << 4));
                }
        }
        __syncthreads();
    }
    stage_x(ws, 0, lds, w, lane);
    const unsigned obase = (mat == 0 ? O_QXF : O_KXF) + (unsigned)(h * 8 + k4) * 1024 + lane * 16;
    for (int s = 0; s < NSEG; ++s) {
        if (s < 3) { asm volatile("s_waitcnt vmcnt(0) lgkmcnt(0)" ::: "memory"); } else { asm volatile("s_waitcnt vmcnt(2) lgkmcnt(0)" ::: "memory"); }
        __builtin_amdgcn_s_barrier();
        if (tid == 0) { if (s > 0 && s < 3) ctr_add(ws, C_QK + s - 1); else if (s > 3) ctr_add(ws, C_QK + s - 2); }
        if (s + 1 < NSEG) stage_x(ws, s + 1, lds + ((s + 1) & 1) * 40960, w, lane);
        LAS unsigned char* xt = lds + (s & 1) * 40960;
        f32x4 acc[2][2];
#pragma unroll
        for (int tt = 0; tt < 2; ++tt) { acc[0][tt] = (f32x4){0, 0, 0, 0}; acc[1][tt] = (f32x4){0, 0, 0, 0}; }
#define QK_BODY(k, x0, x1) do { \
            acc[0][0] = __builtin_amdgcn_mfma_f32_16x16x32_bf16(aw[0][k], x0, acc[0][0], 0, 0, 0); acc[1][0] = __builtin_amdgcn_mfma_f32_16x16x32_bf16(aw[1][k], x0, acc[1][0], 0, 0, 0); \
            acc[0][1] = __builtin_amdgcn_mfma_f32_16x16x32_bf16(aw[0][k], x1, acc[0][1], 0, 0, 0); acc[1][1] = __builtin_amdgcn_mfma_f32_16x16x32_bf16(aw[1][k], x1, acc[1][1], 0, 0, 0); } while (0)
        XPIPE(xt, QK_BODY, 2);
#pragma unroll
        for (int tt = 0; tt < 2; ++tt) st16(rc, obase + (unsigned)s * 262144u + tt * 4096, pack8(acc[0][tt], acc[1][tt]));
    }
    asm volatile("s_waitcnt vmcnt(0)" ::: "memory");
    __syncthreads();
    if (tid == 0) { ctr_add(ws, C_QK + NSEG - 2); ctr_add(ws, C_QK + NSEG - 1); }
}

__device__ __forceinline__ void vo_role(const WS& ws, int h, LAS unsigned char* lds) {
    const int tid = otid(), lane = tid & 63, w = __builtin_amdgcn_readfirstlane(tid >> 6);
    const int g = lane >> 4, c = lane & 15, q4 = c >> 2, p4 = c & 3;
    const int p = w & 3, kh = w >> 2;
    const rsrc_t rc = ws_rsrc(ws);
    bf16x8 av[2][10], bo[5][4];
    {
        auto stage = [&](int rd, int buf) {
#pragma unroll
            for (int i = 0; i < 8; ++i) {
                const int pc = w * 8 + i, m = pc >> 5, row = (pc & 31) * 4 + (lane >> 4);
                if (m == 0) GLDS16(ws.Wv + (size_t)(rd * 128 + row) * HD + h * DH + ((lane & 15) ^ wsw(row)) * 8, lds + buf * 65536 + pc * 1024);
                else GLDS16(ws.Wo + (size_t)(h * DH + row) * D + rd * 128 + ((lane & 15) ^ wsw2(row)) * 8, lds + buf * 65536 + pc * 1024);
            }
        };
        stage(0, 0); stage(1, 1);
#pragma unroll
        for (int rd = 0; rd < 5; ++rd) {
            if (rd > 0 && rd + 1 < 5) { asm volatile("s_waitcnt lgkmcnt(0)" ::: "memory"); __builtin_amdgcn_s_barrier(); stage(rd + 1, (rd + 1) & 1); }
            if (rd + 1 < 5) { asm volatile("s_waitcnt vmcnt(8)" ::: "memory"); } else { asm volatile("s_waitcnt vmcnt(0)" ::: "memory"); }
            __builtin_amdgcn_s_barrier();
            LAS unsigned char* m = lds + (rd & 1) * 65536 + (p4 & 1) * 8;
#pragma unroll
            for (int k2 = 0; k2 < 2; ++k2)
#pragma unroll
                for (int i = 0; i < 2; ++i) {
                    const int r0 = (2 * k2 + kh) * 32 + 8 * g + q4, r1 = r0 + 4, ec = (2 * p + i) * 2 + (p4 >> 1);
                    av[i][rd * 2 + k2] = trfrag(m + r0 * 256 + ((ec ^ wsw(r0)) << 4), m + r1 * 256 + ((ec ^ wsw(r1)) << 4));
                }
            const int ec = w * 2 + (p4 >> 1);
#pragma unroll
            for (int kk = 0; kk < 4; ++kk) {
                const int c0 = kk * 32 + 4 * g + q4, c1 = c0 + 16;
                bo[rd][kk] = trfrag(m + 32768 + c0 * 256 + ((ec ^ wsw2(c0)) << 4), m + 32768 + c1 * 256 + ((ec ^ wsw2(c1)) << 4));
            }
        }
        __syncthreads();
    }
    LAS unsigned char* vf = lds + 81920;
    LAS unsigned char* xch = lds + 90112;
    const int xo0 = c * 1280 + (((4 * kh + g) ^ c) << 4), xo1 = c * 1280 + (((8 + 4 * kh + g) ^ c) << 4);
    stage_x(ws, 0, lds, w, lane);
#pragma unroll 1
    for (int s = 0; s < NSEG; ++s) {
        if (s == 0) { asm volatile("s_waitcnt vmcnt(0) lgkmcnt(0)" ::: "memory"); } else { asm volatile("s_waitcnt vmcnt(5) lgkmcnt(0)" ::: "memory"); }
        __builtin_amdgcn_s_barrier();
        if (s + 1 < NSEG) stage_x(ws, s + 1, lds + ((s + 1) & 1) * 40960, w, lane);
        LAS unsigned char* xt = lds + (s & 1) * 40960;
        f32x4 acc[2][2];
#pragma unroll
        for (int i = 0; i < 2; ++i) { acc[i][0] = (f32x4){0, 0, 0, 0}; acc[i][1] = (f32x4){0, 0, 0, 0}; }
#define XF(m_, tt_) __builtin_bit_cast(bf16x8, *(LAS v4u*)(xt + (((m_) & 1) ? xo1 : xo0) + (tt_) * 20480 + ((m_) >> 1) * 256))
        bf16x8 xb[2][4];
#pragma unroll
        for (int q = 0; q < 2; ++q) { xb[0][2 * q] = XF(q, 0); xb[0][2 * q + 1] = XF(q, 1); }
#pragma unroll
        for (int bt = 0; bt < 5; ++bt) {
            if (bt + 1 < 5) {
#pragma unroll
                for (int q = 0; q < 2; ++q) { xb[(bt + 1) & 1][2 * q] = XF(2 * bt + 2 + q, 0); xb[(bt + 1) & 1][2 * q + 1] = XF(2 * bt + 2 + q, 1); }
            }
            __builtin_amdgcn_sched_barrier(0);
#pragma unroll
            for (int q = 0; q < 2; ++q)
#pragma unroll
                for (int i = 0; i < 2; ++i) {
                    acc[i][0] = __builtin_amdgcn_mfma_f32_16x16x32_bf16(av[i][2 * bt + q], xb[bt & 1][2 * q], acc[i][0], 0, 0, 0);
                    acc[i][1] = __builtin_amdgcn_mfma_f32_16x16x32_bf16(av[i][2 * bt + q], xb[bt & 1][2 * q + 1], acc[i][1], 0, 0, 0);
                }
            __builtin_amdgcn_sched_barrier(0);
        }
        *(LAS f32x4*)(xch + (w * 2 + 0) * 1024 + lane * 16) = kh ? acc[0][0] : acc[0][1];
        *(LAS f32x4*)(xch + (w * 2 + 1) * 1024 + lane * 16) = kh ? acc[1][0] : acc[1][1];
        if (s + 1 < NSEG) { asm volatile("s_waitcnt vmcnt(5) lgkmcnt(0)" ::: "memory"); } else { asm volatile("s_waitcnt vmcnt(0) lgkmcnt(0)" ::: "memory"); }
        __builtin_amdgcn_s_barrier();
        if (tid == 0 && s > 0) ctr_add(ws, C_VW + s - 1);
        {
            f32x4 o0 = *(LAS f32x4*)(xch + ((w ^ 4) * 2 + 0) * 1024 + lane * 16), o1 = *(LAS f32x4*)(xch + ((w ^ 4) * 2 + 1) * 1024 + lane * 16);
            f32x4 m0 = kh ? acc[0][1] : acc[0][0], m1 = kh ? acc[1][1] : acc[1][0];
            *(LAS v4u*)(vf + (kh * 4 + p) * 1024 + lane * 16) = pack8(m0 + o0, m1 + o1);
        }
        asm volatile("s_waitcnt lgkmcnt(0)" ::: "memory");
        __builtin_amdgcn_s_barrier();
        bf16x8 v0[4], v1[4];
#pragma unroll
        for (int kk = 0; kk < 4; ++kk) { v0[kk] = __builtin_bit_cast(bf16x8, *(LAS v4u*)(vf + kk * 1024 + lane * 16)); v1[kk] = __builtin_bit_cast(bf16x8, *(LAS v4u*)(vf + (4 + kk) * 1024 + lane * 16)); }
#pragma unroll
        for (int cc = 0; cc < 5; ++cc) {
            f32x4 d0 = {0, 0, 0, 0}, d1 = {0, 0, 0, 0};
#pragma unroll
            for (int kk = 0; kk < 4; ++kk) {
                d0 = __builtin_amdgcn_mfma_f32_16x16x32_bf16(v0[kk], bo[cc][kk], d0, 0, 0, 0);
                d1 = __builtin_amdgcn_mfma_f32_16x16x32_bf16(v1[kk], bo[cc][kk], d1, 0, 0, 0);
            }
            const int j = (cc * 128 + w * 16 + c) >> 3;
            st16(rc, O_VWF + (unsigned)(((s * 80 + j) * 16 + (h >> 1)) * 64 + ((h & 1) * 8 + (c & 7) + 16 * g)) * 16u, pack8(d0, d1));
        }
    }
    asm volatile("s_waitcnt vmcnt(0)" ::: "memory");
    __syncthreads();
    if (tid == 0) ctr_add(ws, C_VW + NSEG - 1);
}

__device__ __forceinline__ void task_f1s(const WS& ws, int cb, int s0, int ns, LAS unsigned char* lds) {
    const int tid = otid(), lane = tid & 63, w = __builtin_amdgcn_readfirstlane(tid >> 6);
    const int g = lane >> 4, c = lane & 15;
    const rsrc_t rw = ws_rsrc(ws);
    stage_x(ws, s0, lds, w, lane);
    v4u bh[20], bl[20];
    const unsigned fo = O_FF + (unsigned)(cb * 8 + w) * 40960 + lane * 16;
#pragma unroll
    for (int k = 0; k < 20; ++k) { bh[k] = ld16(rw, fo + k * 2048); bl[k] = ld16(rw, fo + k * 2048 + 1024); }
    const int col = cb * 128 + w * 16 + c;
    for (int i = 0; i < ns; ++i) {
        const int s = s0 + i;
        if (i == 0) { asm volatile("s_waitcnt vmcnt(0) lgkmcnt(0)" ::: "memory"); } else { asm volatile("s_waitcnt vmcnt(2) lgkmcnt(0)" ::: "memory"); }
        __builtin_amdgcn_s_barrier();
        if (i + 1 < ns) stage_x(ws, s + 1, lds + ((i + 1) & 1) * 40960, w, lane);
        LAS unsigned char* xt = lds + (i & 1) * 40960;
        f32x4 a0 = {0, 0, 0, 0}, a1 = {0, 0, 0, 0};
#define F1_BODY(k, x0, x1) do { \
            a0 = __builtin_amdgcn_mfma_f32_16x16x32_bf16(x0, __builtin_bit_cast(bf16x8, bl[k]), a0, 0, 0, 0); a1 = __builtin_amdgcn_mfma_f32_16x16x32_bf16(x1, __builtin_bit_cast(bf16x8, bl[k]), a1, 0, 0, 0); \
            a0 = __builtin_amdgcn_mfma_f32_16x16x32_bf16(x0, __builtin_bit_cast(bf16x8, bh[k]), a0, 0, 0, 0); a1 = __builtin_amdgcn_mfma_f32_16x16x32_bf16(x1, __builtin_bit_cast(bf16x8, bh[k]), a1, 0, 0, 0); } while (0)
        XPIPE(xt, F1_BODY, 2);
        st16(rw, O_XT + (unsigned)(col * 2048 + s * 32 + 4 * g) * 4u, __builtin_bit_cast(v4u, a0));
        st16(rw, O_XT + (unsigned)(col * 2048 + s * 32 + 16 + 4 * g) * 4u, __builtin_bit_cast(v4u, a1));
    }
}

__device__ __forceinline__ void task_f3s(const WS& ws, int db, int s0, int ns, LAS unsigned char* lds) {
    const int tid = otid(), lane = tid & 63, w = __builtin_amdgcn_readfirstlane(tid >> 6);
    const int g = lane >> 4, c = lane & 15, q4 = c >> 2, p4 = c & 3;
    const rsrc_t rw = ws_rsrc(ws);
    v4u gh[20], gl[20];
    const unsigned go = O_GF + (unsigned)(db * 8 + w) * 40960 + lane * 16;
#pragma unroll
    for (int k = 0; k < 20; ++k) { gh[k] = ld16(rw, go + k * 2048); gl[k] = ld16(rw, go + k * 2048 + 1024); }
    v4u st[5];
    const int pl = tid >> 8, t8 = tid & 255;
    const unsigned gsrc = (pl ? O_YL : O_YH) + t8 * 16;
    LAS unsigned char* ldst = lds + pl * 20480 + (t8 >> 2) * 64 + (((t8 & 3) ^ (((t8 >> 5) & 1) << 1)) << 4);
    auto gload = [&](int s, int hf) {
#pragma unroll
        for (int i = 0; i < 5; ++i) st[i] = ld16(rw, gsrc + (unsigned)s * 40960u + hf * 20480 + i * 4096);
    };
    auto lwrite = [&](int buf) {
#pragma unroll
        for (int i = 0; i < 5; ++i) *(LAS v4u*)(ldst + buf * 40960 + i * 4096) = st[i];
    };
    gload(s0, 0); lwrite(0); gload(s0, 1);
    __syncthreads();
    const float sc = 1.0f / (2048.0f * 640.0f);
    for (int i = 0; i < ns; ++i) {
        const int s = s0 + i;
        f32x4 a0 = {0, 0, 0, 0}, a1 = {0, 0, 0, 0};
#pragma unroll
        for (int hf = 0; hf < 2; ++hf) {
            LAS unsigned char* base = lds + hf * 40960;
#pragma unroll
            for (int kk = 0; kk < 10; ++kk) {
                const int k = hf * 10 + kk;
                const int r0 = kk * 32 + 8 * g + q4, r1 = r0 + 4, sw = (g & 1) << 5;
                LAS unsigned char* m0 = base + r0 * 64 + p4 * 8; LAS unsigned char* m1 = base + r1 * 64 + p4 * 8;
                bf16x8 yh0 = trfrag(m0 + (0 ^ sw), m1 + (0 ^ sw)), yh1 = trfrag(m0 + (32 ^ sw), m1 + (32 ^ sw));
                bf16x8 yl0 = trfrag(m0 + 20480 + (0 ^ sw), m1 + 20480 + (0 ^ sw)), yl1 = trfrag(m0 + 20480 + (32 ^ sw), m1 + 20480 + (32 ^ sw));
                a0 = __builtin_amdgcn_mfma_f32_16x16x32_bf16(__builtin_bit_cast(bf16x8, gl[k]), yh0, a0, 0, 0, 0);
                a1 = __builtin_amdgcn_mfma_f32_16x16x32_bf16(__builtin_bit_cast(bf16x8, gl[k]), yh1, a1, 0, 0, 0);
                a0 = __builtin_amdgcn_mfma_f32_16x16x32_bf16(__builtin_bit_cast(bf16x8, gh[k]), yl0, a0, 0, 0, 0);
                a1 = __builtin_amdgcn_mfma_f32_16x16x32_bf16(__builtin_bit_cast(bf16x8, gh[k]), yl1, a1, 0, 0, 0);
                a0 = __builtin_amdgcn_mfma_f32_16x16x32_bf16(__builtin_bit_cast(bf16x8, gh[k]), yh0, a0, 0, 0, 0);
                a1 = __builtin_amdgcn_mfma_f32_16x16x32_bf16(__builtin_bit_cast(bf16x8, gh[k]), yh1, a1, 0, 0, 0);
                if (kk & 1) __builtin_amdgcn_sched_barrier(0);
            }
            if (hf == 0) { lwrite(1); if (i + 1 < ns) gload(s + 1, 0); }
            else if (i + 1 < ns) { lwrite(0); gload(s + 1, 1); }
            __syncthreads();
        }
        const int d0 = db * 128 + w * 16 + 4 * g;
        v2u o0 = {pk2(a0[0] * sc, a0[1] * sc), pk2(a0[2] * sc, a0[3] * sc)}, o1 = {pk2(a1[0] * sc, a1[1] * sc), pk2(a1[2] * sc, a1[3] * sc)};
        __builtin_amdgcn_raw_buffer_store_b64(o0, rw, O_ABUF + (unsigned)((s * 32 + c) * D + d0) * 2u, 0, 16);
        __builtin_amdgcn_raw_buffer_store_b64(o1, rw, O_ABUF + (unsigned)((s * 32 + 16 + c) * D + d0) * 2u, 0, 16);
    }
}

__device__ __forceinline__ void task_wt2(const WS& ws, int tb, LAS unsigned char* lds) {
    const int tid = otid(), lane = tid & 63, w = __builtin_amdgcn_readfirstlane(tid >> 6);
    const rsrc_t rw = ws_rsrc(ws);
    LAS unsigned* tile = (LAS unsigned*)lds;
#pragma unroll
    for (int i = 0; i < 8; ++i) {
        const int e = w * 8 + i, tl = e >> 4, hf = (e >> 3) & 1, k2 = e & 7;
        const int p = (tb * 4 + tl) + 128 * hf, k0 = p >> 4, k1 = p & 15, u = k0 + 16 * k1 + 256 * k2;
#pragma unroll
        for (int q = 0; q < 6; ++q) {
            const int f = q * 64 + lane;
            if (f < DF) tile[f * 68 + e] = (unsigned)ws.wr[u * DF + f] | ((unsigned)ws.wi[u * DF + f] << 16);
        }
    }
    __syncthreads();
    for (int q = tid; q < DF * 16; q += 512) {
        const int f = q >> 4, ch = q & 15;
        st16(rw, O_WT + (unsigned)f * 8192 + tb * 256 + ch * 16, *(LAS v4u*)(tile + f * 68 + ch * 4));
    }
    __syncthreads();
}

__device__ __forceinline__ void task_attB2(const WS& ws, int s, int jg, int f3c, LAS unsigned char* lds) {
    const int tid = otid(), lane = tid & 63, w = __builtin_amdgcn_readfirstlane(tid >> 6);
    const int g = lane >> 4, c = lane & 15, hh = c >> 3, t8 = c & 7;
    LAS unsigned char* px = lds; LAS unsigned char* pm = lds + 65536; LAS float* gate = (LAS float*)(lds + 81920);
    const rsrc_t rw = ws_rsrc(ws);
    const int j = jg * 8 + w;
    wait_ctr(rw, C_VW + s, 32); wait_ctr(rw, f3c, 5);
    v4u av[16]; v2u aa[4];
    {
        const unsigned vw = O_VWF + (unsigned)(s * 80 + j) * 16384 + lane * 16;
#pragma unroll
        for (int t2 = 0; t2 < 16; ++t2) av[t2] = ld16(rw, vw + t2 * 1024);
#pragma unroll
        for (int tg = 0; tg < 4; ++tg) aa[tg] = ld8(rw, O_ABUF + (unsigned)((s * 32 + tg * 8 + t8) * D + j * 8 + (g & 1) * 4) * 2u);
        const int tok = tid >> 4, part = tid & 15;
        float acc = 0;
#pragma unroll
        for (int i = 0; i < 5; ++i) {
            v4u xv = *(const v4u*)(ws.x + (size_t)(s * 32 + tok) * D + (i * 16 + part) * 8), gv = *(const v4u*)(ws.gw + (i * 16 + part) * 8);
#pragma unroll
            for (int q = 0; q < 4; ++q) { unsigned pr = pk2(l16f(xv[q]) * l16f(gv[q]), h16f(xv[q]) * h16f(gv[q])); acc += l16f(pr) + h16f(pr); }
        }
#pragma unroll
        for (int o = 1; o < 16; o <<= 1) acc += __shfl_xor(acc, o);
        if (part == 0) { float z = rbf(acc); gate[tok] = rbf(1.0f / (1.0f + __expf(-z))); }
    }
    wait_ctr(rw, C_ATTA + s, 64);
    {
        const unsigned src = O_PXF + (unsigned)s * 65536 + tid * 16;
        v4u t[8];
#pragma unroll
        for (int i = 0; i < 8; ++i) t[i] = ld16(rw, src + i * 8192);
        const unsigned src2 = O_PMF + (unsigned)s * 16384 + tid * 16;
        v4u u0 = ld16(rw, src2), u1 = ld16(rw, src2 + 8192);
#pragma unroll
        for (int i = 0; i < 8; ++i) *(LAS v4u*)(px + i * 8192 + tid * 16) = t[i];
        *(LAS v4u*)(pm + tid * 16) = u0; *(LAS v4u*)(pm + 8192 + tid * 16) = u1;
    }
    wait_prog(rw, s + 2);
    v2u au[8];
    {
        const unsigned uf = O_UF + (unsigned)(s * 80 + j) * 4096 + lane * 8;
#pragma unroll
        for (int t2 = 0; t2 < 8; ++t2) au[t2] = ld8(rw, uf + t2 * 512);
    }
    f32x4 acc[4], acc2[4];
#pragma unroll
    for (int tg = 0; tg < 4; ++tg) { acc[tg] = (f32x4){0, 0, 0, 0}; acc2[tg] = (f32x4){0, 0, 0, 0}; }
#pragma unroll
    for (int t = 0; t < 16; ++t)
#pragma unroll
        for (int tg = 0; tg < 4; ++tg) {
            v4u bp = *(LAS v4u*)(px + (((2 * t + hh) * 2 + (tg >> 1)) * 64 + (tg & 1) * 8 + t8 + 16 * g) * 16);
            acc[tg] = __builtin_amdgcn_mfma_f32_16x16x32_bf16(__builtin_bit_cast(bf16x8, av[t]), __builtin_bit_cast(bf16x8, bp), acc[tg], 0, 0, 0);
        }
#pragma unroll
    for (int t = 0; t < 8; ++t)
#pragma unroll
        for (int tg = 0; tg < 4; ++tg) {
            v2u bp = *(LAS v2u*)(pm + (((4 * t + 2 * (g >> 1) + hh) * 32 + tg * 8 + t8) * 16 + (g & 1) * 8));
            acc2[tg] = __builtin_amdgcn_mfma_f32_16x16x16bf16_1k(__builtin_bit_cast(s16x4, au[t]), __builtin_bit_cast(s16x4, bp), acc2[tg], 0, 0, 0);
        }
#pragma unroll
    for (int tg = 0; tg < 4; ++tg) {
        f32x4 o = acc[tg] + acc2[tg];
        float r4[4];
#pragma unroll
        for (int r = 0; r < 4; ++r) {
            unsigned hi = __builtin_amdgcn_permlane32_swap(__float_as_uint(o[r]), __float_as_uint(o[r]), false, false)[1];
            unsigned sh = __builtin_amdgcn_update_dpp(0u, hi, 0x108, 0xf, 0xf, false);
            r4[r] = o[r] + __uint_as_float(sh);
        }
        if (g < 2 && hh == 0) {
            const size_t off = (size_t)(s * 32 + tg * 8 + t8) * D + j * 8 + g * 4;
            const float gt = gate[tg * 8 + t8], om = rbf(1.0f - gt);
            const v2u av2 = aa[tg];
            unsigned b01 = pk2(r4[0], r4[1]), b23 = pk2(r4[2], r4[3]);
            unsigned ga01 = pk2(gt * l16f(av2.x), gt * h16f(av2.x)), ga23 = pk2(gt * l16f(av2.y), gt * h16f(av2.y));
            unsigned ob01 = pk2(om * l16f(b01), om * h16f(b01)), ob23 = pk2(om * l16f(b23), om * h16f(b23));
            v2u res = {pk2(l16f(ga01) + l16f(ob01), h16f(ga01) + h16f(ob01)), pk2(l16f(ga23) + l16f(ob23), h16f(ga23) + h16f(ob23))};
            *(v2u*)(ws.out + off) = res;
        }
    }
    __syncthreads();
}

__device__ __forceinline__ unsigned xcc_id() { return (unsigned)__builtin_amdgcn_s_getreg(20 | (3 << 11)) & 0xfu; }

constexpr int F1Q = 16, F3Q = 16;
constexpr int G_WVO = 0, G_DFT = 0, G_WT = G_DFT + 50, G_F1 = G_WT + 32, G_F2 = G_F1 + 5 * F1Q, G_F3 = G_F2 + 81, G_ATT = G_F3 + 5 * F3Q;
constexpr int ATT_PER = 10;
constexpr int G_END = G_ATT + NSEG * ATT_PER;
constexpr int NSTATIC = 82;

__device__ __forceinline__ void signal(rsrc_t rc, const WS& ws, unsigned ctr) {
    asm volatile("s_waitcnt vmcnt(0)" ::: "memory");
    __syncthreads();
    if (threadIdx.x == 0) ctr_add(ws, ctr);
}

__device__ __forceinline__ void worker(const WS& ws, int first, LAS unsigned char* lds, int maxit) {
    const int tid = threadIdx.x;
    const rsrc_t rc = ws_rsrc(ws);
    LAS int* slot = (LAS int*)(lds + LDS_BYTES - 64);
    bool table = false;
    for (int iter = 0; iter < maxit; ++iter) {
        int t;
        if (maxit == 1 && first < 0) break;
        if (iter == 0 && first >= 0) t = first;
        else {
            if (tid == 0) { int q = NSTATIC + (int)ctr_add(ws, C_GHEAD); *slot = q < G_END ? q : -1; }
            __syncthreads();
            t = __builtin_amdgcn_readfirstlane(*slot);
            __syncthreads();
        }
        if (t < 0) break;
        if (t < G_WT) { for (int i = 0; i < 4; ++i) wave_dftmat(ws, ((t - G_DFT) * 4 + i) * 8 + (tid >> 6)); signal(rc, ws, C_DFT); }
        else if (t < G_F1) { task_wt2(ws, t - G_WT, lds); signal(rc, ws, C_WT); table = false; }
        else if (t < G_F2) {
            const int q = t - G_F1;
            wait_ctr(rc, C_DFT, 50);
            task_f1s(ws, q % 5, (q / 5) * (64 / F1Q), 64 / F1Q, lds); signal(rc, ws, C_F1); table = false;
        } else if (t < G_F3) {
            wait_ctr(rc, C_F1, 5 * F1Q); wait_ctr(rc, C_WT, 32);
            task_f2(ws, t - G_F2, lds, !table); table = true; signal(rc, ws, C_F2);
        } else if (t < G_ATT) {
            const int q = t - G_F3;
            wait_ctr(rc, C_F2, 81);
            task_f3s(ws, q % 5, (q / 5) * (64 / F3Q), 64 / F3Q, lds); signal(rc, ws, C_F3 + q / 5); table = false;
        } else {
            const int q = t - G_ATT, s = q / ATT_PER, r = q % ATT_PER;
            task_attB2(ws, s, r, C_F3 + s / (64 / F3Q), lds); table = false;
        }
    }
}

__global__ void __launch_bounds__(512, 2) k_mega(WS ws, ChainCfg cfg) {
    extern __shared__ __attribute__((aligned(16))) unsigned char smem[];
    LAS unsigned char* lds = (LAS unsigned char*)smem;
    LAS int* slot = (LAS int*)(lds + LDS_BYTES - 64);
    const rsrc_t rc = ws_rsrc(ws);
    if (threadIdx.x == 0) {
        const unsigned x = xcc_id() & 7u;
        const unsigned t = ctr_add(ws, C_XCD + x);
        int sl = (int)(x * 32 + t);
        if (t >= 32) {
            const unsigned k = ctr_add(ws, C_OVF);
            unsigned spins = 0;
            for (;;) {
                unsigned tot = 0; for (int i = 0; i < 8; ++i) tot += ctr_get(rc, C_XCD + i);
                if (tot >= gridDim.x) break;
                __builtin_amdgcn_s_sleep(8); asm volatile("" ::: "memory");
                if (++spins > (1u << 22)) { set_err(rc, 9); break; }
            }
            unsigned acc = 0; sl = -1;
            for (int i = 0; i < 8; ++i) { const unsigned n = ctr_get(rc, C_XCD + i); const unsigned fr = n < 32 ? 32 - n : 0; if (sl < 0 && k < acc + fr) sl = i * 32 + (int)(n + (k - acc)); acc += fr; }
        }
        *slot = sl;
    }
    __syncthreads();
    const int sl = __builtin_amdgcn_readfirstlane(*slot);
    __syncthreads();
    if (sl < 0) return;
    const int x = sl >> 5, t = sl & 31;
    int first = -1;
    if (x < 4) {
        if (t < 8) { head_role(ws, cfg, x * 8 + t, lds); return; }
        if (t < 16) { u_role(ws, cfg, x * 8 + (t - 8), lds); return; }
        if (t < 26) { st2_role(ws, cfg, x * 10 + (t - 16), lds); return; }
        if (sl == 31) { sched_role(ws, cfg); return; }
        first = 64 + x * 6 + (t - 26) - (x > 0 ? 1 : 0);
    } else {
        if (t < 8) qk_role(ws, (x - 4) * 8 + t, lds);
        else if (t < 16) vo_role(ws, (x - 4) * 8 + (t - 8), lds);
        else first = (x - 4) * 16 + (t - 16);
        if (t < 16 && (cfg.fl & 1)) return;
    }
    if (first >= 0) until_rt(rt32() + cfg.fl2);
    if (first >= NSTATIC) first = -1;
    worker(ws, first, lds, (x < 4 && (cfg.fl & 2)) ? 1 : 1 << 30);
}

extern "C" void kernel_launch(void* const* d_in, const int* in_sizes, int n_in, void* d_out, int out_size, void* d_ws, size_t ws_size, hipStream_t stream) {
    WS ws;
    ws.x = (const u16*)d_in[0]; ws.wr = (const u16*)d_in[1]; ws.wi = (const u16*)d_in[2]; ws.mem0 = (const u16*)d_in[4];
    ws.Wq = (const u16*)d_in[5]; ws.Wk = (const u16*)d_in[6]; ws.Wv = (const u16*)d_in[7]; ws.Wo = (const u16*)d_in[8]; ws.gw = (const u16*)d_in[10];
    ws.out = (u16*)d_out; ws.wsb = (unsigned char*)d_ws;
    (void)hipFuncSetAttribute((const void*)k_mega, hipFuncAttributeMaxDynamicSharedMemorySize, LDS_BYTES);
    (void)hipMemsetAsync(d_ws, 0xFF, CTL_END, stream);
#define CFG_T1 90
#define CFG_T2 80
#define CFG_T3 144
#define CFG_PAD 120
#define CFG_D0 -6
#define CFG_PEN 28
#define CFG_SD1 60
#define CFG_SD2 60
#define CFG_FL 2
    ChainCfg cfg{CFG_T1, CFG_T2, CFG_T3, CFG_PAD, CFG_D0, CFG_PEN, CFG_SD1, CFG_SD2, CFG_FL, 500};
    k_mega<<<256, 512, LDS_BYTES, stream>>>(ws, cfg);
}
```

```cpp
constexpr unsigned O_FF = O_FFT;
constexpr unsigned O_GF = O_FF + 40 * 20 * 2 * 1024;
constexpr unsigned O_WT = O_GF + 40 * 20 * 2 * 1024;
constexpr unsigned O_XT = O_WT + 324 * 2048 * 4;
constexpr unsigned O_YH = O_XT + 640 * 2048 * 4;
constexpr unsigned O_YL = O_YH + 640 * 2048 * 2;
constexpr unsigned O_FFT_END = O_YL + 640 * 2048 * 2;

#define split2(a, b, hi, lo) do { unsigned h_ = pk2(a, b); hi = h_; lo = pk2((a) - l16f(h_), (b) - h16f(h_)); } while (0)

__device__ __forceinline__ void wave_dftmat(const WS& ws, int item) {
    const int lane = otid() & 63, g = lane >> 4, c = lane & 15;
    const int which = item / 800, tile = (item % 800) / 20, k = item % 20;
    const rsrc_t rw = ws_rsrc(ws);
    float v[8];
#pragma unroll
    for (int j = 0; j < 8; ++j) {
        int n, d;
        if (which == 0) { n = 16 * tile + c; d = 32 * k + 8 * g + j; }
        else { n = 32 * k + 8 * g + j; d = 16 * tile + c; }
        const bool im = n > 320; const int f = im ? n - 320 : n;
        const int idx = (f * d) % D;
        float sn, cs; sincospif((float)idx * (2.0f / D), &sn, &cs);
        float val = im ? -sn : cs;
        if (which == 1 && f != 0 && f != 320) val *= 2.0f;
        v[j] = val;
    }
    v4u hi, lo;
    split2(v[0], v[1], hi.x, lo.x); split2(v[2], v[3], hi.y, lo.y); split2(v[4], v[5], hi.z, lo.z); split2(v[6], v[7], hi.w, lo.w);
    const unsigned o = (which ? O_GF : O_FF) + (unsigned)((tile * 20 + k) * 2) * 1024 + lane * 16;
    st16(rw, o, hi); st16(rw, o + 1024, lo);
}

__device__ __forceinline__ void task_wt(const WS& ws, int fb, LAS unsigned char* lds) {
    const int tid = otid();
    const rsrc_t rw = ws_rsrc(ws);
    LAS unsigned* tile = (LAS unsigned*)lds;
    const int f0 = fb * 16;
    for (int q = tid; q < 2048 * 16; q += 512) {
        const int u = q >> 4, fl = q & 15, f = f0 + fl;
        unsigned v = 0;
        if (f < DF) v = (unsigned)ws.wr[u * DF + f] | ((unsigned)ws.wi[u * DF + f] << 16);
        const int k0 = u & 15, k1 = (u >> 4) & 15, k2 = u >> 8, p = k0 * 16 + k1, t = p & 127, half = p >> 7;
        tile[fl * 2052 + t * 16 + half * 8 + k2] = v;
    }
    __syncthreads();
    for (int q = tid; q < 16 * 512; q += 512) {
        const int fl = q >> 9, ch = q & 511;
        if (f0 + fl < 324) st16(rw, O_WT + (unsigned)(f0 + fl) * 8192 + ch * 16, *(LAS v4u*)(tile + fl * 2052 + ch * 4));
    }
    __syncthreads();
}

__device__ __forceinline__ void task_f1(const WS& ws, int b, int cb) {
    const int tid = otid(), lane = tid & 63, w = __builtin_amdgcn_readfirstlane(tid >> 6);
    const int g = lane >> 4, c = lane & 15;
    const rsrc_t rw = ws_rsrc(ws);
    const int th = w & 1, cg = w >> 1;
    f32x4 acc[4][2];
#pragma unroll
    for (int t = 0; t < 4; ++t) { acc[t][0] = (f32x4){0, 0, 0, 0}; acc[t][1] = (f32x4){0, 0, 0, 0}; }
    const u16* xa = ws.x + (size_t)(b * 128 + th * 64 + c) * D + 8 * g;
    const unsigned fo = O_FF + (unsigned)(cb * 8 + cg * 2) * 40960 + lane * 16;
#pragma unroll 2
    for (int k = 0; k < 20; ++k) {
        v4u a[4], bh[2], bl[2];
#pragma unroll
        for (int t = 0; t < 4; ++t) a[t] = *(const v4u*)(xa + (size_t)t * 16 * D + k * 32);
#pragma unroll
        for (int n = 0; n < 2; ++n) { bh[n] = ld16(rw, fo + n * 40960 + k * 2048); bl[n] = ld16(rw, fo + n * 40960 + k * 2048 + 1024); }
#pragma unroll
        for (int t = 0; t < 4; ++t)
#pragma unroll
            for (int n = 0; n < 2; ++n) {
                acc[t][n] = __builtin_amdgcn_mfma_f32_16x16x32_bf16(__builtin_bit_cast(bf16x8, a[t]), __builtin_bit_cast(bf16x8, bh[n]), acc[t][n], 0, 0, 0);
                acc[t][n] = __builtin_amdgcn_mfma_f32_16x16x32_bf16(__builtin_bit_cast(bf16x8, a[t]), __builtin_bit_cast(bf16x8, bl[n]), acc[t][n], 0, 0, 0);
            }
    }
#pragma unroll
    for (int t = 0; t < 4; ++t)
#pragma unroll
        for (int n = 0; n < 2; ++n) {
            const int col = cb * 128 + (cg * 2 + n) * 16 + c, s0 = b * 128 + th * 64 + t * 16 + 4 * g;
            st16(rw, O_XT + (unsigned)(col * 2048 + s0) * 4u, __builtin_bit_cast(v4u, acc[t][n]));
        }
}

struct cf { float x, y; };
__device__ __forceinline__ cf cadd(cf a, cf b) { return {a.x + b.x, a.y + b.y}; }
__device__ __forceinline__ cf csub(cf a, cf b) { return {a.x - b.x, a.y - b.y}; }
__device__ __forceinline__ cf cmul(cf a, cf b) { return {a.x * b.x - a.y * b.y, a.x * b.y + a.y * b.x}; }
template <int DIR> __device__ __forceinline__ cf mulI(cf a) { return DIR < 0 ? cf{a.y, -a.x} : cf{-a.y, a.x}; }
template <int DIR> __device__ __forceinline__ void dft4(cf& a, cf& b, cf& c, cf& d) {
    cf s0 = cadd(a, c), s1 = csub(a, c), s2 = cadd(b, d), s3 = mulI<DIR>(csub(b, d));
    a = cadd(s0, s2); c = csub(s0, s2); b = cadd(s1, s3); d = csub(s1, s3);
}
template <int DIR> __device__ __forceinline__ cf tw16(cf a, int k) {
    const float C1 = 0.92387953251128674f, S1 = 0.38268343236508977f, R = 0.70710678118654752f;
    const float cs[10] = {1.f, C1, R, S1, 0.f, -S1, -R, -C1, -1.f, -C1}, sn[10] = {0.f, S1, R, C1, 1.f, C1, R, S1, 0.f, -S1};
    if (k == 0) return a;
    const float c = cs[k], s = DIR * sn[k];
    return {a.x * c - a.y * s, a.x * s + a.y * c};
}
template <int DIR> __device__ __forceinline__ void dft16(cf (&v)[16]) {
#pragma unroll
    for (int i = 0; i < 4; ++i) dft4<DIR>(v[i], v[i + 4], v[i + 8], v[i + 12]);
#pragma unroll
    for (int i = 1; i < 4; ++i)
#pragma unroll
        for (int k1 = 1; k1 < 4; ++k1) v[i + 4 * k1] = tw16<DIR>(v[i + 4 * k1], i * k1);
#pragma unroll
    for (int k1 = 0; k1 < 4; ++k1) dft4<DIR>(v[4 * k1], v[4 * k1 + 1], v[4 * k1 + 2], v[4 * k1 + 3]);
#pragma unroll
    for (int a = 0; a < 4; ++a)
#pragma unroll
        for (int b = a + 1; b < 4; ++b) { cf t = v[4 * a + b]; v[4 * a + b] = v[4 * b + a]; v[4 * b + a] = t; }
}
template <int DIR> __device__ __forceinline__ void dft8(cf* v) {
    dft4<DIR>(v[0], v[2], v[4], v[6]); dft4<DIR>(v[1], v[3], v[5], v[7]);
    v[3] = tw16<DIR>(v[3], 2); v[5] = tw16<DIR>(v[5], 4); v[7] = tw16<DIR>(v[7], 6);
    cf o[8];
#pragma unroll
    for (int k1 = 0; k1 < 4; ++k1) { o[k1] = cadd(v[2 * k1], v[2 * k1 + 1]); o[k1 + 4] = csub(v[2 * k1], v[2 * k1 + 1]); }
#pragma unroll
    for (int i = 0; i < 8; ++i) v[i] = o[i];
}
__device__ __forceinline__ int fpad(int i) { return i + (i >> 4); }
__device__ __forceinline__ void task_f2(const WS& ws, int fg, LAS unsigned char* lds, bool make_table) {
    const int tid = otid(), col = tid >> 7, t = tid & 127;
    const rsrc_t rw = ws_rsrc(ws);
    LAS f32x2v* tw = (LAS f32x2v*)lds;
    LAS f32x2v* buf = (LAS f32x2v*)(lds + 16384) + col * 2176;
    if (make_table) {
        for (int i = tid; i < 2048; i += 512) { float sn, cs; sincospif((float)i * (1.0f / 1024.0f), &sn, &cs); tw[i] = (f32x2v){cs, sn}; }
    }
    const int f = fg * 4 + col;
    const bool valid = f < DF, hasim = valid && f != 0 && f != 320;
    const int fr = valid ? f : 0;
    cf v[16];
#pragma unroll
    for (int s2 = 0; s2 < 16; ++s2) {
        v[s2].x = __uint_as_float(__builtin_amdgcn_raw_buffer_load_b32(rw, O_XT + (unsigned)(fr * 2048 + s2 * 128 + t) * 4u, 0, 16));
        v[s2].y = hasim ? __uint_as_float(__builtin_amdgcn_raw_buffer_load_b32(rw, O_XT + (unsigned)((320 + fr) * 2048 + s2 * 128 + t) * 4u, 0, 16)) : 0.f;
    }
    v4u wv[4];
#pragma unroll
    for (int i = 0; i < 4; ++i) wv[i] = ld16(rw, O_WT + (unsigned)fr * 8192 + t * 64 + i * 16);
    __syncthreads();
    dft16<-1>(v);
#pragma unroll
    for (int k0 = 1; k0 < 16; ++k0) { f32x2v w = tw[k0 * t]; v[k0] = cmul(v[k0], cf{w.x, -w.y}); }
#pragma unroll
    for (int k0 = 0; k0 < 16; ++k0) buf[fpad(k0 * 128 + t)] = (f32x2v){v[k0].x, v[k0].y};
    __syncthreads();
    { const int k0 = t >> 3, s0 = t & 7;
#pragma unroll
      for (int s1 = 0; s1 < 16; ++s1) { f32x2v q = buf[fpad(k0 * 128 + s1 * 8 + s0)]; v[s1] = cf{q.x, q.y}; }
      dft16<-1>(v);
#pragma unroll
      for (int k1 = 1; k1 < 16; ++k1) { f32x2v w = tw[k1 * s0 * 16]; v[k1] = cmul(v[k1], cf{w.x, -w.y}); }
      __syncthreads();
#pragma unroll
      for (int k1 = 0; k1 < 16; ++k1) buf[fpad((k0 * 16 + k1) * 8 + s0)] = (f32x2v){v[k1].x, v[k1].y};
    }
    __syncthreads();
#pragma unroll
    for (int hf = 0; hf < 2; ++hf) {
#pragma unroll
        for (int s0 = 0; s0 < 8; ++s0) { f32x2v q = buf[fpad((t + 128 * hf) * 8 + s0)]; v[hf * 8 + s0] = cf{q.x, q.y}; }
        dft8<-1>(v + hf * 8);
#pragma unroll
        for (int k2 = 0; k2 < 8; ++k2) { const unsigned ww = wv[hf * 2 + (k2 >> 2)][k2 & 3]; v[hf * 8 + k2] = cmul(v[hf * 8 + k2], cf{l16f(ww), h16f(ww)}); }
        dft8<1>(v + hf * 8);
#pragma unroll
        for (int s0 = 0; s0 < 8; ++s0) buf[fpad((t + 128 * hf) * 8 + s0)] = (f32x2v){v[hf * 8 + s0].x, v[hf * 8 + s0].y};
    }
    __syncthreads();
    { const int k0 = t >> 3, s0 = t & 7;
#pragma unroll
      for (int k1 = 0; k1 < 16; ++k1) { f32x2v q = buf[fpad((k0 * 16 + k1) * 8 + s0)]; v[k1] = cf{q.x, q.y}; }
#pragma unroll
      for (int k1 = 1; k1 < 16; ++k1) { f32x2v w = tw[k1 * s0 * 16]; v[k1] = cmul(v[k1], cf{w.x, w.y}); }
      dft16<1>(v);
      __syncthreads();
#pragma unroll
      for (int s1 = 0; s1 < 16; ++s1) buf[fpad(k0 * 128 + s1 * 8 + s0)] = (f32x2v){v[s1].x, v[s1].y};
    }
    __syncthreads();
#pragma unroll
    for (int k0 = 0; k0 < 16; ++k0) { f32x2v q = buf[fpad(k0 * 128 + t)]; v[k0] = cf{q.x, q.y}; }
#pragma unroll
    for (int k0 = 1; k0 < 16; ++k0) { f32x2v w = tw[k0 * t]; v[k0] = cmul(v[k0], cf{w.x, w.y}); }
    dft16<1>(v);
    __syncthreads();
#pragma unroll
    for (int s2 = 0; s2 < 16; ++s2) buf[fpad(s2 * 128 + t)] = (f32x2v){v[s2].x, v[s2].y};
    __syncthreads();
#pragma unroll
    for (int hf = 0; hf < 2; ++hf) {
        const int s0 = 8 * (t + 128 * hf);
        f32x2v q[8];
#pragma unroll
        for (int i = 0; i < 8; ++i) q[i] = buf[fpad(s0 + i)];
        v4u rh, rl, ih, il;
        split2(q[0].x, q[1].x, rh.x, rl.x); split2(q[2].x, q[3].x, rh.y, rl.y); split2(q[4].x, q[5].x, rh.z, rl.z); split2(q[6].x, q[7].x, rh.w, rl.w);
        split2(q[0].y, q[1].y, ih.x, il.x); split2(q[2].y, q[3].y, ih.y, il.y); split2(q[4].y, q[5].y, ih.z, il.z); split2(q[6].y, q[7].y, ih.w, il.w);
        const unsigned yo = (unsigned)(((s0 >> 5) * 640 + f) * 32 + (s0 & 31)) * 2u;
        if (valid) { st16(rw, O_YH + yo, rh); st16(rw, O_YL + yo, rl); }
        if (hasim) { st16(rw, O_YH + yo + 320 * 64, ih); st16(rw, O_YL + yo + 320 * 64, il); }
    }
    __syncthreads();
}

__device__ __forceinline__ void stage_x(const WS& ws, int s, LAS unsigned char* dst, int w, int lane) {
#pragma unroll
    for (int i = 0; i < 5; ++i) {
        const int pc = w * 5 + i, q = pc * 64 + lane, row = q / 80, slot = q - row * 80;
        glds16_asm(ws.x + (size_t)(s * 32 + row) * D + (slot ^ (row & 15)) * 8, dst + pc * 1024);
    }
}
__device__ __forceinline__ bf16x8 xfrag(LAS unsigned char* xt, int tt, int k, int c, int g) {
    return __builtin_bit_cast(bf16x8, *(LAS v4u*)(xt + (c * 1280 + (((4 * (k & 3) + g) ^ c) << 4)) + (tt * 20480 + (k >> 2) * 256)));
}

#define XPIPE(xt, BODY, NB) do { \
    bf16x8 xb_[2][2 * NB]; \
    _Pragma("unroll") for (int q_ = 0; q_ < NB; ++q_) { xb_[0][2 * q_] = xfrag(xt, 0, q_, c, g); xb_[0][2 * q_ + 1] = xfrag(xt, 1, q_, c, g); } \
    __builtin_amdgcn_sched_barrier(0); \
    _Pragma("unroll") for (int b_ = 0; b_ < 20 / NB; ++b_) { \
        BODY(NB * b_, xb_[b_ & 1][0], xb_[b_ & 1][1]); \
        __builtin_amdgcn_sched_barrier(0); \
        if (b_ + 1 < 20 / NB) { _Pragma("unroll") for (int q_ = 0; q_ < NB; ++q_) { xb_[(b_ + 1) & 1][2 * q_] = xfrag(xt, 0, NB * b_ + NB + q_, c, g); xb_[(b_ + 1) & 1][2 * q_ + 1] = xfrag(xt, 1, NB * b_ + NB + q_, c, g); } } \
        __builtin_amdgcn_sched_barrier(0); \
        _Pragma("unroll") for (int q_ = 1; q_ < NB; ++q_) { BODY(NB * b_ + q_, xb_[b_ & 1][2 * q_], xb_[b_ & 1][2 * q_ + 1]); } \
        __builtin_amdgcn_sched_barrier(0); \
    } } while (0)

__device__ __forceinline__ void qk_role(const WS& ws, int h, LAS unsigned char* lds) {
    const int tid = otid(), lane = tid & 63, w = __builtin_amdgcn_readfirstlane(tid >> 6);
    const int g = lane >> 4, c = lane & 15, q4 = c >> 2, p4 = c & 3;
    const rsrc_t rc = ws_rsrc(ws);
    const int mat = w >> 2, k4 = w & 3;
    bf16x8 aw[2][20];
    {
        auto stage = [&](int rd, int buf) {
#pragma unroll
            for (int i = 0; i < 8; ++i) {
                const int pc = w * 8 + i, m = pc >> 5, row = (pc & 31) * 4 + (lane >> 4), ch = (lane & 15) ^ wsw(row);
                const u16* W = m ? ws.Wk : ws.Wq;
                GLDS16(W + (size_t)(rd * 128 + row) * HD + h * DH + ch * 8, lds + buf * 65536 + pc * 1024);
            }
        };
        stage(0, 0); stage(1, 1);
#pragma unroll
        for (int rd = 0; rd < 5; ++rd) {
            if (rd > 0 && rd + 1 < 5) { asm volatile("s_waitcnt lgkmcnt(0)" ::: "memory"); __builtin_amdgcn_s_barrier(); stage(rd + 1, (rd + 1) & 1); }
            if (rd + 1 < 5) { asm volatile("s_waitcnt vmcnt(8)" ::: "memory"); } else { asm volatile("s_waitcnt vmcnt(0)" ::: "memory"); }
            __builtin_amdgcn_s_barrier();
#pragma unroll
            for (int kk = 0; kk < 4; ++kk)
#pragma unroll
                for (int i = 0; i < 2; ++i) {
                    const int r0 = kk * 32 + 8 * g + q4, r1 = r0 + 4, ec = (2 * k4 + i) * 2 + (p4 >> 1);
                    LAS unsigned char* m = lds + (rd & 1) * 65536 + mat * 32768 + (p4 & 1) * 8;
                    aw[i][rd * 4 + kk] = trfrag(m + r0 * 256 + ((ec ^ wsw(r0)) << 4), m + r1 * 256 + ((ec ^ wsw(r1)) << 4));
                }
        }
        __syncthreads();
    }
    stage_x(ws, 0, lds, w, lane);
    const unsigned obase = (mat == 0 ? O_QXF : O_KXF) + (unsigned)(h * 8 + k4) * 1024 + lane * 16;
    for (int s = 0; s < NSEG; ++s) {
        if (s < 3) { asm volatile("s_waitcnt vmcnt(0) lgkmcnt(0)" ::: "memory"); } else { asm volatile("s_waitcnt vmcnt(2) lgkmcnt(0)" ::: "memory"); }
        __builtin_amdgcn_s_barrier();
        if (tid == 0) { if (s > 0 && s < 3) ctr_add(ws, C_QK + s - 1); else if (s > 3) ctr_add(ws, C_QK + s - 2); }
        if (s + 1 < NSEG) stage_x(ws, s + 1, lds + ((s + 1) & 1) * 40960, w, lane);
        LAS unsigned char* xt = lds + (s & 1) * 40960;
        f32x4 acc[2][2];
#pragma unroll
        for (int tt = 0; tt < 2; ++tt) { acc[0][tt] = (f32x4){0, 0, 0, 0}; acc[1][tt] = (f32x4){0, 0, 0, 0}; }
#define QK_BODY(k, x0, x1) do { \
            acc[0][0] = __builtin_amdgcn_mfma_f32_16x16x32_bf16(aw[0][k], x0, acc[0][0], 0, 0, 0); acc[1][0] = __builtin_amdgcn_mfma_f32_16x16x32_bf16(aw[1][k], x0, acc[1][0], 0, 0, 0); \
            acc[0][1] = __builtin_amdgcn_mfma_f32_16x16x32_bf16(aw[0][k], x1, acc[0][1], 0, 0, 0); acc[1][1] = __builtin_amdgcn_mfma_f32_16x16x32_bf16(aw[1][k], x1, acc[1][1], 0, 0, 0); } while (0)
        XPIPE(xt, QK_BODY, 2);
#pragma unroll
        for (int tt = 0; tt < 2; ++tt) st16(rc, obase + (unsigned)s * 262144u + tt * 4096, pack8(acc[0][tt], acc[1][tt]));
    }
    asm volatile("s_waitcnt vmcnt(0)" ::: "memory");
    __syncthreads();
    if (tid == 0) { ctr_add(ws, C_QK + NSEG - 2); ctr_add(ws, C_QK + NSEG - 1); }
}

__device__ __forceinline__ void vo_role(const WS& ws, int h, LAS unsigned char* lds) {
    const int tid = otid(), lane = tid & 63, w = __builtin_amdgcn_readfirstlane(tid >> 6);
    const int g = lane >> 4, c = lane & 15, q4 = c >> 2, p4 = c & 3;
    const int p = w & 3, kh = w >> 2;
    const rsrc_t rc = ws_rsrc(ws);
    bf16x8 av[2][10], bo[5][4];
    {
        auto stage = [&](int rd, int buf) {
#pragma unroll
            for (int i = 0; i < 8; ++i) {
                const int pc = w * 8 + i, m = pc >> 5, row = (pc & 31) * 4 + (lane >> 4);
                if (m == 0) GLDS16(ws.Wv + (size_t)(rd * 128 + row) * HD + h * DH + ((lane & 15) ^ wsw(row)) * 8, lds + buf * 65536 + pc * 1024);
                else GLDS16(ws.Wo + (size_t)(h * DH + row) * D + rd * 128 + ((lane & 15) ^ wsw2(row)) * 8, lds + buf * 65536 + pc * 1024);
            }
        };
        stage(0, 0); stage(1, 1);
#pragma unroll
        for (int rd = 0; rd < 5; ++rd) {
            if (rd > 0 && rd + 1 < 5) { asm volatile("s_waitcnt lgkmcnt(0)" ::: "memory"); __builtin_amdgcn_s_barrier(); stage(rd + 1, (rd + 1) & 1); }
            if (rd + 1 < 5) { asm volatile("s_waitcnt vmcnt(8)" ::: "memory"); } else { asm volatile("s_waitcnt vmcnt(0)" ::: "memory"); }
            __builtin_amdgcn_s_barrier();
            LAS unsigned char* m = lds + (rd & 1) * 65536 + (p4 & 1) * 8;
#pragma unroll
            for (int k2 = 0; k2 < 2; ++k2)
#pragma unroll
                for (int i = 0; i < 2; ++i) {
                    const int r0 = (2 * k2 + kh) * 32 + 8 * g + q4, r1 = r0 + 4, ec = (2 * p + i) * 2 + (p4 >> 1);
                    av[i][rd * 2 + k2] = trfrag(m + r0 * 256 + ((ec ^ wsw(r0)) << 4), m + r1 * 256 + ((ec ^ wsw(r1)) << 4));
                }
            const int ec = w * 2 + (p4 >> 1);
#pragma unroll
            for (int kk = 0; kk < 4; ++kk) {
                const int c0 = kk * 32 + 4 * g + q4, c1 = c0 + 16;
                bo[rd][kk] = trfrag(m + 32768 + c0 * 256 + ((ec ^ wsw2(c0)) << 4), m + 32768 + c1 * 256 + ((ec ^ wsw2(c1)) << 4));
            }
        }
        __syncthreads();
    }
    const Pol pol = wait_pol(rc);
    const unsigned vwo = vw_off((unsigned)((h >> 1) * 64 + ((h & 1) * 8 + (c & 7) + 16 * g)) * 16u);
    LAS unsigned char* vf = lds + 81920;
    LAS unsigned char* xch = lds + 90112;
    const int xo0 = c * 1280 + (((4 * kh + g) ^ c) << 4), xo1 = c * 1280 + (((8 + 4 * kh + g) ^ c) << 4);
    stage_x(ws, 0, lds, w, lane);
#pragma unroll 1
    for (int s = 0; s < NSEG; ++s) {
        if (s <= 1) { asm volatile("s_waitcnt vmcnt(0) lgkmcnt(0)" ::: "memory"); } else { asm volatile("s_waitcnt vmcnt(5) lgkmcnt(0)" ::: "memory"); }
        __builtin_amdgcn_s_barrier();
        if (tid == 0 && s == 1) ctr_add(ws, C_VW);
        if (s + 1 < NSEG) stage_x(ws, s + 1, lds + ((s + 1) & 1) * 40960, w, lane);
        LAS unsigned char* xt = lds + (s & 1) * 40960;
        f32x4 acc[2][2];
#pragma unroll
        for (int i = 0; i < 2; ++i) { acc[i][0] = (f32x4){0, 0, 0, 0}; acc[i][1] = (f32x4){0, 0, 0, 0}; }
#define XF(m_, tt_) __builtin_bit_cast(bf16x8, *(LAS v4u*)(xt + (((m_) & 1) ? xo1 : xo0) + (tt_) * 20480 + ((m_) >> 1) * 256))
        bf16x8 xb[2][4];
#pragma unroll
        for (int q = 0; q < 2; ++q) { xb[0][2 * q] = XF(q, 0); xb[0][2 * q + 1] = XF(q, 1); }
#pragma unroll
        for (int bt = 0; bt < 5; ++bt) {
            if (bt + 1 < 5) {
#pragma unroll
                for (int q = 0; q < 2; ++q) { xb[(bt + 1) & 1][2 * q] = XF(2 * bt + 2 + q, 0); xb[(bt + 1) & 1][2 * q + 1] = XF(2 * bt + 2 + q, 1); }
            }
            __builtin_amdgcn_sched_barrier(0);
#pragma unroll
            for (int q = 0; q < 2; ++q)
#pragma unroll
                for (int i = 0; i < 2; ++i) {
                    acc[i][0] = __builtin_amdgcn_mfma_f32_16x16x32_bf16(av[i][2 * bt + q], xb[bt & 1][2 * q], acc[i][0], 0, 0, 0);
                    acc[i][1] = __builtin_amdgcn_mfma_f32_16x16x32_bf16(av[i][2 * bt + q], xb[bt & 1][2 * q + 1], acc[i][1], 0, 0, 0);
                }
            __builtin_amdgcn_sched_barrier(0);
        }
        *(LAS f32x4*)(xch + (w * 2 + 0) * 1024 + lane * 16) = kh ? acc[0][0] : acc[0][1];
        *(LAS f32x4*)(xch + (w * 2 + 1) * 1024 + lane * 16) = kh ? acc[1][0] : acc[1][1];
        if (s + 1 < NSEG) { asm volatile("s_waitcnt vmcnt(5) lgkmcnt(0)" ::: "memory"); } else { asm volatile("s_waitcnt vmcnt(0) lgkmcnt(0)" ::: "memory"); }
        __builtin_amdgcn_s_barrier();
        if (tid == 0 && s > 1) ctr_add(ws, C_VW + s - 1);
        {
            f32x4 o0 = *(LAS f32x4*)(xch + ((w ^ 4) * 2 + 0) * 1024 + lane * 16), o1 = *(LAS f32x4*)(xch + ((w ^ 4) * 2 + 1) * 1024 + lane * 16);
            f32x4 m0 = kh ? acc[0][1] : acc[0][0], m1 = kh ? acc[1][1] : acc[1][0];
            *(LAS v4u*)(vf + (kh * 4 + p) * 1024 + lane * 16) = pack8(m0 + o0, m1 + o1);
        }
        asm volatile("s_waitcnt lgkmcnt(0)" ::: "memory");
        __builtin_amdgcn_s_barrier();
        bf16x8 v0[4], v1[4];
#pragma unroll
        for (int kk = 0; kk < 4; ++kk) { v0[kk] = __builtin_bit_cast(bf16x8, *(LAS v4u*)(vf + kk * 1024 + lane * 16)); v1[kk] = __builtin_bit_cast(bf16x8, *(LAS v4u*)(vf + (4 + kk) * 1024 + lane * 16)); }
#pragma unroll
        for (int cc = 0; cc < 5; ++cc) {
            f32x4 d0 = {0, 0, 0, 0}, d1 = {0, 0, 0, 0};
#pragma unroll
            for (int kk = 0; kk < 4; ++kk) {
                d0 = __builtin_amdgcn_mfma_f32_16x16x32_bf16(v0[kk], bo[cc][kk], d0, 0, 0, 0);
                d1 = __builtin_amdgcn_mfma_f32_16x16x32_bf16(v1[kk], bo[cc][kk], d1, 0, 0, 0);
            }
            const int j = (cc * 128 + w * 16 + c) >> 3;
            st16(rc, vw_base(pol, s, j) + vwo, pack8(d0, d1));
        }
    }
    asm volatile("s_waitcnt vmcnt(0)" ::: "memory");
    __syncthreads();
    if (tid == 0) ctr_add(ws, C_VW + NSEG - 1);
}

__device__ __forceinline__ void task_f1s(const WS& ws, int cb, int s0, int ns, LAS unsigned char* lds) {
    const int tid = otid(), lane = tid & 63, w = __builtin_amdgcn_readfirstlane(tid >> 6);
    const int g = lane >> 4, c = lane & 15;
    const rsrc_t rw = ws_rsrc(ws);
    stage_x(ws, s0, lds, w, lane);
    v4u bh[20], bl[20];
    const unsigned fo = O_FF + (unsigned)(cb * 8 + w) * 40960 + lane * 16;
#pragma unroll
    for (int k = 0; k < 20; ++k) { bh[k] = ld16(rw, fo + k * 2048); bl[k] = ld16(rw, fo + k * 2048 + 1024); }
    const int col = cb * 128 + w * 16 + c;
    for (int i = 0; i < ns; ++i) {
        const int s = s0 + i;
        if (i == 0) { asm volatile("s_waitcnt vmcnt(0) lgkmcnt(0)" ::: "memory"); } else { asm volatile("s_waitcnt vmcnt(2) lgkmcnt(0)" ::: "memory"); }
        __builtin_amdgcn_s_barrier();
        if (i + 1 < ns) stage_x(ws, s + 1, lds + ((i + 1) & 1) * 40960, w, lane);
        LAS unsigned char* xt = lds + (i & 1) * 40960;
        f32x4 a0 = {0, 0, 0, 0}, a1 = {0, 0, 0, 0};
#define F1_BODY(k, x0, x1) do { \
            a0 = __builtin_amdgcn_mfma_f32_16x16x32_bf16(x0, __builtin_bit_cast(bf16x8, bl[k]), a0, 0, 0, 0); a1 = __builtin_amdgcn_mfma_f32_16x16x32_bf16(x1, __builtin_bit_cast(bf16x8, bl[k]), a1, 0, 0, 0); \
            a0 = __builtin_amdgcn_mfma_f32_16x16x32_bf16(x0, __builtin_bit_cast(bf16x8, bh[k]), a0, 0, 0, 0); a1 = __builtin_amdgcn_mfma_f32_16x16x32_bf16(x1, __builtin_bit_cast(bf16x8, bh[k]), a1, 0, 0, 0); } while (0)
        XPIPE(xt, F1_BODY, 2);
        st16(rw, O_XT + (unsigned)(col * 2048 + s * 32 + 4 * g) * 4u, __builtin_bit_cast(v4u, a0));
        st16(rw, O_XT + (unsigned)(col * 2048 + s * 32 + 16 + 4 * g) * 4u, __builtin_bit_cast(v4u, a1));
    }
}

__device__ __forceinline__ void task_f3s(const WS& ws, int db, int s0, int ns, LAS unsigned char* lds) {
    const int tid = otid(), lane = tid & 63, w = __builtin_amdgcn_readfirstlane(tid >> 6);
    const int g = lane >> 4, c = lane & 15, q4 = c >> 2, p4 = c & 3;
    const rsrc_t rw = ws_rsrc(ws);
    v4u gh[20], gl[20];
    const unsigned go = O_GF + (unsigned)(db * 8 + w) * 40960 + lane * 16;
#pragma unroll
    for (int k = 0; k < 20; ++k) { gh[k] = ld16(rw, go + k * 2048); gl[k] = ld16(rw, go + k * 2048 + 1024); }
    wait_ctr(rw, C_F2, 81);
    v4u st[5];
    const int pl = tid >> 8, t8 = tid & 255;
    const unsigned gsrc = (pl ? O_YL : O_YH) + t8 * 16;
    LAS unsigned char* ldst = lds + pl * 20480 + (t8 >> 2) * 64 + (((t8 & 3) ^ (((t8 >> 5) & 1) << 1)) << 4);
    auto gload = [&](int s, int hf) {
#pragma unroll
        for (int i = 0; i < 5; ++i) st[i] = ld16(rw, gsrc + (unsigned)s * 40960u + hf * 20480 + i * 4096);
    };
    auto lwrite = [&](int buf) {
#pragma unroll
        for (int i = 0; i < 5; ++i) *(LAS v4u*)(ldst + buf * 40960 + i * 4096) = st[i];
    };
    gload(s0, 0); lwrite(0); gload(s0, 1);
    __syncthreads();
    const float sc = 1.0f / (2048.0f * 640.0f);
    for (int i = 0; i < ns; ++i) {
        const int s = s0 + i;
        f32x4 a0 = {0, 0, 0, 0}, a1 = {0, 0, 0, 0};
#pragma unroll
        for (int hf = 0; hf < 2; ++hf) {
            LAS unsigned char* base = lds + hf * 40960;
#pragma unroll
            for (int kk = 0; kk < 10; ++kk) {
                const int k = hf * 10 + kk;
                const int r0 = kk * 32 + 8 * g + q4, r1 = r0 + 4, sw = (g & 1) << 5;
                LAS unsigned char* m0 = base + r0 * 64 + p4 * 8; LAS unsigned char* m1 = base + r1 * 64 + p4 * 8;
                bf16x8 yh0 = trfrag(m0 + (0 ^ sw), m1 + (0 ^ sw)), yh1 = trfrag(m0 + (32 ^ sw), m1 + (32 ^ sw));
                bf16x8 yl0 = trfrag(m0 + 20480 + (0 ^ sw), m1 + 20480 + (0 ^ sw)), yl1 = trfrag(m0 + 20480 + (32 ^ sw), m1 + 20480 + (32 ^ sw));
                a0 = __builtin_amdgcn_mfma_f32_16x16x32_bf16(__builtin_bit_cast(bf16x8, gl[k]), yh0, a0, 0, 0, 0);
                a1 = __builtin_amdgcn_mfma_f32_16x16x32_bf16(__builtin_bit_cast(bf16x8, gl[k]), yh1, a1, 0, 0, 0);
                a0 = __builtin_amdgcn_mfma_f32_16x16x32_bf16(__builtin_bit_cast(bf16x8, gh[k]), yl0, a0, 0, 0, 0);
                a1 = __builtin_amdgcn_mfma_f32_16x16x32_bf16(__builtin_bit_cast(bf16x8, gh[k]), yl1, a1, 0, 0, 0);
                a0 = __builtin_amdgcn_mfma_f32_16x16x32_bf16(__builtin_bit_cast(bf16x8, gh[k]), yh0, a0, 0, 0, 0);
                a1 = __builtin_amdgcn_mfma_f32_16x16x32_bf16(__builtin_bit_cast(bf16x8, gh[k]), yh1, a1, 0, 0, 0);
                if (kk & 1) __builtin_amdgcn_sched_barrier(0);
            }
            if (hf == 0) { lwrite(1); if (i + 1 < ns) gload(s + 1, 0); }
            else if (i + 1 < ns) { lwrite(0); gload(s + 1, 1); }
            __syncthreads();
        }
        const int d0 = db * 128 + w * 16 + 4 * g;
        v2u o0 = {pk2(a0[0] * sc, a0[1] * sc), pk2(a0[2] * sc, a0[3] * sc)}, o1 = {pk2(a1[0] * sc, a1[1] * sc), pk2(a1[2] * sc, a1[3] * sc)};
        __builtin_amdgcn_raw_buffer_store_b64(o0, rw, O_ABUF + (unsigned)((s * 32 + c) * D + d0) * 2u, 0, 16);
        __builtin_amdgcn_raw_buffer_store_b64(o1, rw, O_ABUF + (unsigned)((s * 32 + 16 + c) * D + d0) * 2u, 0, 16);
    }
}

__device__ __forceinline__ void task_wt2(const WS& ws, int tb, LAS unsigned char* lds) {
    const int tid = otid(), lane = tid & 63, w = __builtin_amdgcn_readfirstlane(tid >> 6);
    const rsrc_t rw = ws_rsrc(ws);
    LAS unsigned* tile = (LAS unsigned*)lds;
#pragma unroll
    for (int i = 0; i < 8; ++i) {
        const int e = w * 8 + i, tl = e >> 4, hf = (e >> 3) & 1, k2 = e & 7;
        const int p = (tb * 4 + tl) + 128 * hf, k0 = p >> 4, k1 = p & 15, u = k0 + 16 * k1 + 256 * k2;
#pragma unroll
        for (int q = 0; q < 6; ++q) {
            const int f = q * 64 + lane;
            if (f < DF) tile[f * 68 + e] = (unsigned)ws.wr[u * DF + f] | ((unsigned)ws.wi[u * DF + f] << 16);
        }
    }
    __syncthreads();
    for (int q = tid; q < DF * 16; q += 512) {
        const int f = q >> 4, ch = q & 15;
        st16(rw, O_WT + (unsigned)f * 8192 + tb * 256 + ch * 16, *(LAS v4u*)(tile + f * 68 + ch * 4));
    }
    __syncthreads();
}

template <int NTG>
__device__ __forceinline__ void task_attB2(const WS& ws, int s, int jg, int tg0, int f3c, LAS unsigned char* lds) {
    const int tid = otid(), lane = tid & 63, w = __builtin_amdgcn_readfirstlane(tid >> 6);
    const int g = lane >> 4, c = lane & 15, hh = c >> 3, t8 = c & 7;
    LAS unsigned char* px = lds; LAS unsigned char* pm = lds + 65536; LAS float* gate = (LAS float*)(lds + 81920);
    const rsrc_t rw = ws_rsrc(ws);
    const int j = jg * 8 + w;
    wait_ctr(rw, C_VW + s, 32); wait_ctr(rw, f3c, 5);
    v4u av[16]; v2u aa[NTG];
    {
        Pol pol; pol.m0 = ctr_get(rw, C_POL) - 1u; pol.m1 = ctr_get(rw, C_POL + 1) - 1u; pol.m2 = ctr_get(rw, C_POL + 2) - 1u;
        const unsigned vw = vw_base(pol, s, j) + lane * 16;
#pragma unroll
        for (int t2 = 0; t2 < 16; ++t2) av[t2] = ld16(rw, vw + vw_off(t2 * 1024));
#pragma unroll
        for (int tq = 0; tq < NTG; ++tq) aa[tq] = ld8(rw, O_ABUF + (unsigned)((s * 32 + (tg0 + tq) * 8 + t8) * D + j * 8 + (g & 1) * 4) * 2u);
        const int tok = tid >> 4, part = tid & 15;
        float acc = 0;
#pragma unroll
        for (int i = 0; i < 5; ++i) {
            v4u xv = *(const v4u*)(ws.x + (size_t)(s * 32 + tok) * D + (i * 16 + part) * 8), gv = *(const v4u*)(ws.gw + (i * 16 + part) * 8);
#pragma unroll
            for (int q = 0; q < 4; ++q) { unsigned pr = pk2(l16f(xv[q]) * l16f(gv[q]), h16f(xv[q]) * h16f(gv[q])); acc += l16f(pr) + h16f(pr); }
        }
#pragma unroll
        for (int o = 1; o < 16; o <<= 1) acc += __shfl_xor(acc, o);
        if (part == 0) { float z = rbf(acc); gate[tok] = rbf(1.0f / (1.0f + __expf(-z))); }
    }
    wait_ctr(rw, C_ATTA + s, 64);
    if (NTG == 4) {
        const unsigned src = O_PXF + (unsigned)s * 65536 + tid * 16;
        v4u t[8];
#pragma unroll
        for (int i = 0; i < 8; ++i) t[i] = ld16(rw, src + i * 8192);
        const unsigned src2 = O_PMF + (unsigned)s * 16384 + tid * 16;
        v4u u0 = ld16(rw, src2), u1 = ld16(rw, src2 + 8192);
#pragma unroll
        for (int i = 0; i < 8; ++i) *(LAS v4u*)(px + i * 8192 + tid * 16) = t[i];
        *(LAS v4u*)(pm + tid * 16) = u0; *(LAS v4u*)(pm + 8192 + tid * 16) = u1;
    } else {
        const int o0 = (((tid >> 5) * 2 + (tg0 >> 1)) * 64 + (tg0 & 1) * 8 + (tid & 7) + 16 * ((tid >> 3) & 3)) * 16;
        const int o2 = (((tid >> 3) & 31) * 32 + tg0 * 8 + (tid & 7)) * 16;
        v4u t0 = ld16(rw, O_PXF + (unsigned)s * 65536 + o0), t1 = ld16(rw, O_PXF + (unsigned)s * 65536 + o0 + 32768), u0 = ld16(rw, O_PMF + (unsigned)s * 16384 + o2);
        *(LAS v4u*)(px + o0) = t0; *(LAS v4u*)(px + o0 + 32768) = t1; *(LAS v4u*)(pm + o2) = u0;
    }
    __syncthreads();
    f32x4 acc[NTG], acc2[NTG];
#pragma unroll
    for (int tq = 0; tq < NTG; ++tq) { acc[tq] = (f32x4){0, 0, 0, 0}; acc2[tq] = (f32x4){0, 0, 0, 0}; }
    const bool early = ctr_get(rw, C_PROG) >= (unsigned)(s + 2);
    const unsigned uf = O_UF + (unsigned)(s * 80 + j) * 4096 + (2 * (g >> 1) + hh) * 128 + (g & 1) * 64 + t8 * 8;
    v2u au[8];
    if (early) {
#pragma unroll
        for (int t2 = 0; t2 < 8; ++t2) au[t2] = ld8(rw, uf + t2 * 512);
    }
#pragma unroll
    for (int t = 0; t < 16; ++t)
#pragma unroll
        for (int tq = 0; tq < NTG; ++tq) {
            const int tg = tg0 + tq;
            v4u bp = *(LAS v4u*)(px + (((2 * t + hh) * 2 + (tg >> 1)) * 64 + (tg & 1) * 8 + t8 + 16 * g) * 16);
            acc[tq] = __builtin_amdgcn_mfma_f32_16x16x32_bf16(__builtin_bit_cast(bf16x8, av[t]), __builtin_bit_cast(bf16x8, bp), acc[tq], 0, 0, 0);
        }
    if (!early) {
        wave_wait_ctr(rw, C_PROG, (unsigned)(s + 2));
#pragma unroll
        for (int t2 = 0; t2 < 8; ++t2) au[t2] = ld8(rw, uf + t2 * 512);
    }
#pragma unroll
    for (int t = 0; t < 8; ++t)
#pragma unroll
        for (int tq = 0; tq < NTG; ++tq) {
            const int tg = tg0 + tq;
            v2u bp = *(LAS v2u*)(pm + (((4 * t + 2 * (g >> 1) + hh) * 32 + tg * 8 + t8) * 16 + (g & 1) * 8));
            acc2[tq] = __builtin_amdgcn_mfma_f32_16x16x16bf16_1k(__builtin_bit_cast(s16x4, au[t]), __builtin_bit_cast(s16x4, bp), acc2[tq], 0, 0, 0);
        }
#pragma unroll
    for (int tq = 0; tq < NTG; ++tq) {
        const int tg = tg0 + tq;
        f32x4 o = acc[tq] + acc2[tq];
        float r4[4];
#pragma unroll
        for (int r = 0; r < 4; ++r) {
            unsigned hi = __builtin_amdgcn_permlane32_swap(__float_as_uint(o[r]), __float_as_uint(o[r]), false, false)[1];
            unsigned sh = __builtin_amdgcn_update_dpp(0u, hi, 0x108, 0xf, 0xf, false);
            r4[r] = o[r] + __uint_as_float(sh);
        }
        if (g < 2 && hh == 0) {
            const size_t off = (size_t)(s * 32 + tg * 8 + t8) * D + j * 8 + g * 4;
            const float gt = gate[tg * 8 + t8], om = rbf(1.0f - gt);
            const v2u av2 = aa[tq];
            unsigned b01 = pk2(r4[0], r4[1]), b23 = pk2(r4[2], r4[3]);
            unsigned ga01 = pk2(gt * l16f(av2.x), gt * h16f(av2.x)), ga23 = pk2(gt * l16f(av2.y), gt * h16f(av2.y));
            unsigned ob01 = pk2(om * l16f(b01), om * h16f(b01)), ob23 = pk2(om * l16f(b23), om * h16f(b23));
            v2u res = {pk2(l16f(ga01) + l16f(ob01), h16f(ga01) + h16f(ob01)), pk2(l16f(ga23) + l16f(ob23), h16f(ga23) + h16f(ob23))};
            *(v2u*)(ws.out + off) = res;
        }
    }
    __syncthreads();
}

__device__ __forceinline__ unsigned xcc_id() { return (unsigned)__builtin_amdgcn_s_getreg(20 | (3 << 11)) & 0xfu; }

constexpr int F1Q = 12, F3Q = 12, NF2FAR = 59;
constexpr int G_WVO = 0, G_DFT = 0, G_WT = G_DFT + 50, G_F1 = G_WT + 32, G_F2 = G_F1 + 5 * F1Q, G_F3 = G_F2 + NF2FAR, G_ATT = G_F3 + 5 * F3Q;
constexpr int ATT_PER = 10;
constexpr int NFINE = 2;
constexpr int G_FINE = G_ATT + (NSEG - NFINE) * ATT_PER, G_END = G_FINE + NFINE * ATT_PER * 4;
constexpr int NSTATIC = 82;

__device__ __forceinline__ void signal(rsrc_t rc, const WS& ws, unsigned ctr) {
    asm volatile("s_waitcnt vmcnt(0)" ::: "memory");
    __syncthreads();
    if (threadIdx.x == 0) ctr_add(ws, ctr);
}

__device__ __forceinline__ void worker(const WS& ws, int first, LAS unsigned char* lds, int maxit, int second) {
    const int tid = threadIdx.x;
    const rsrc_t rc = ws_rsrc(ws);
    LAS int* slot = (LAS int*)(lds + LDS_BYTES - 64);
    bool table = false;
    for (int iter = 0; iter < maxit; ++iter) {
        int t;
        if (second >= 0 && iter == 0 && first < 0) continue;
        if (iter == 0 && first >= 0) t = first;
        else if (second >= 0) t = second;
        else {
            if (tid == 0) { int q = NSTATIC + (int)ctr_add(ws, C_GHEAD); *slot = q < G_END ? q : -1; }
            __syncthreads();
            t = __builtin_amdgcn_readfirstlane(*slot);
            __syncthreads();
        }
        if (t < 0) break;
        if (t < G_WT) { for (int i = 0; i < 4; ++i) wave_dftmat(ws, ((t - G_DFT) * 4 + i) * 8 + (tid >> 6)); signal(rc, ws, C_DFT); }
        else if (t < G_F1) { task_wt2(ws, t - G_WT, lds); signal(rc, ws, C_WT); table = false; }
        else if (t < G_F2) {
            const int q = t - G_F1;
            wait_ctr(rc, C_DFT, 50);
            { const int p = q / 5, s0 = 64 * p / F1Q; task_f1s(ws, q % 5, s0, 64 * (p + 1) / F1Q - s0, lds); } signal(rc, ws, C_F1); table = false;
        } else if (t < G_F3 || t >= 50000) {
            wait_ctr(rc, C_F1, 5 * F1Q); wait_ctr(rc, C_WT, 32);
            task_f2(ws, t >= 50000 ? t - 50000 : t - G_F2, lds, !table); table = true; signal(rc, ws, C_F2);
        } else if (t < G_ATT) {
            const int q = t - G_F3;
            wait_ctr(rc, C_DFT, 50);
            { const int p = q / 5, s0 = 64 * p / F3Q; task_f3s(ws, q % 5, s0, 64 * (p + 1) / F3Q - s0, lds); } signal(rc, ws, C_F3 + q / 5); table = false;
        } else if (t < G_FINE) {
            const int q = t - G_ATT, s = q / ATT_PER, r = q % ATT_PER;
            task_attB2<4>(ws, s, r, 0, C_F3 + (F3Q * s + F3Q - 1) / 64, lds); table = false;
        } else {
            const int q = t - G_FINE, s = NSEG - NFINE + q / (ATT_PER * 4), r = q % (ATT_PER * 4);
            task_attB2<1>(ws, s, r >> 2, r & 3, C_F3 + (F3Q * s + F3Q - 1) / 64, lds); table = false;
        }
    }
}

__global__ void __launch_bounds__(512, 2) k_mega(WS ws, ChainCfg cfg) {
    extern __shared__ __attribute__((aligned(16))) unsigned char smem[];
    LAS unsigned char* lds = (LAS unsigned char*)smem;
    LAS int* slot = (LAS int*)(lds + LDS_BYTES - 64);
    const rsrc_t rc = ws_rsrc(ws);
    if (threadIdx.x == 0) {
        const unsigned x = xcc_id() & 7u;
        const unsigned t = ctr_add(ws, C_XCD + x);
        int sl = (int)(x * 32 + t);
        if (t >= 32) {
            const unsigned k = ctr_add(ws, C_OVF);
            unsigned spins = 0;
            for (;;) {
                unsigned tot = 0; for (int i = 0; i < 8; ++i) tot += ctr_get(rc, C_XCD + i);
                if (tot >= gridDim.x) break;
                __builtin_amdgcn_s_sleep(8); asm volatile("" ::: "memory");
                if (++spins > (1u << 22)) { set_err(rc, 9); break; }
            }
            unsigned acc = 0; sl = -1;
            for (int i = 0; i < 8; ++i) { const unsigned n = ctr_get(rc, C_XCD + i); const unsigned fr = n < 32 ? 32 - n : 0; if (sl < 0 && k < acc + fr) sl = i * 32 + (int)(n + (k - acc)); acc += fr; }
        }
        *slot = sl;
    }
    __syncthreads();
    const int sl = __builtin_amdgcn_readfirstlane(*slot);
    __syncthreads();
    if (sl < 0) return;
    const int x = sl >> 5, t = sl & 31;
    int first = -1, nearid = 0;
    if (x < 4) {
        if (t < 8) { head_role(ws, cfg, x * 8 + t, lds); return; }
        if (t < 16) { u_role(ws, cfg, x * 8 + (t - 8), lds); return; }
        if (t < 26) { st2_role(ws, cfg, x * 10 + (t - 16), lds); return; }
        if (sl == 31) { sched_role(ws, cfg, lds); return; }
        nearid = x * 6 + (t - 26) - (x > 0 ? 1 : 0);
        first = 64 + nearid;
    } else {
        if (t < 8) qk_role(ws, (x - 4) * 8 + t, lds);
        else if (t < 16) vo_role(ws, (x - 4) * 8 + (t - 8), lds);
        else first = (x - 4) * 16 + (t - 16);
    }
    if (first >= 0) until_rt(rt32() + cfg.fl2);
    if (first >= NSTATIC) first = -1;
    if (x < 4 && nearid >= 81 - NF2FAR) { if (first < 0) return; }
    worker(ws, first, lds, x < 4 ? (nearid < 81 - NF2FAR ? 2 : 1) : 1 << 30, (x < 4 && nearid < 81 - NF2FAR) ? 50000 + NF2FAR + nearid : -1);
}

extern "C" void kernel_launch(void* const* d_in, const int* in_sizes, int n_in, void* d_out, int out_size, void* d_ws, size_t ws_size, hipStream_t stream) {
    WS ws;
    ws.x = (const u16*)d_in[0]; ws.wr = (const u16*)d_in[1]; ws.wi = (const u16*)d_in[2]; ws.mem0 = (const u16*)d_in[4];
    ws.Wq = (const u16*)d_in[5]; ws.Wk = (const u16*)d_in[6]; ws.Wv = (const u16*)d_in[7]; ws.Wo = (const u16*)d_in[8]; ws.gw = (const u16*)d_in[10];
    ws.out = (u16*)d_out; ws.wsb = (unsigned char*)d_ws;
    (void)hipFuncSetAttribute((const void*)k_mega, hipFuncAttributeMaxDynamicSharedMemorySize, LDS_BYTES);
    (void)hipMemsetAsync(d_ws, 0xFF, CTL_END, stream);
#define CFG_T1 86
#define CFG_T2 80
#define CFG_T3 140
#define CFG_PAD 120
#define CFG_D0 -6
#define CFG_PEN 28
#define CFG_SD1 60
#define CFG_SD2 18
#define CFG_FL 2
#define CFG_M2 2
    ChainCfg cfg{CFG_T1, CFG_T2, CFG_T3, CFG_PAD, CFG_D0, CFG_PEN, CFG_SD1, CFG_SD2, CFG_FL, 500, CFG_M2, 0};
    k_mega<<<256, 512, LDS_BYTES, stream>>>(ws, cfg);
}
```

```cpp
#include <hip/hip_runtime.h>
#include <hip/hip_bf16.h>
#include <stdint.h>
typedef unsigned short u16;
typedef unsigned v4u __attribute__((ext_vector_type(4)));
typedef unsigned v2u __attribute__((ext_vector_type(2)));
typedef __bf16 bf16x4 __attribute__((ext_vector_type(4)));
typedef __bf16 bf16x8 __attribute__((ext_vector_type(8)));
typedef __bf16 bf16x16 __attribute__((ext_vector_type(16)));
typedef short s16x4 __attribute__((ext_vector_type(4)));
typedef float f32x4 __attribute__((ext_vector_type(4)));
typedef unsigned v8u __attribute__((ext_vector_type(8)));
#define LAS __attribute__((address_space(3)))

__device__ __forceinline__ float bf2f(u16 v) { return __uint_as_float(((unsigned)v) << 16); }
__device__ __forceinline__ u16 f2bf(float f) { __hip_bfloat16 b = __float2bfloat16(f); return *reinterpret_cast<u16*>(&b); }
__device__ __forceinline__ float rbf(float f) { return bf2f(f2bf(f)); }
typedef __bf16 bf16x2 __attribute__((ext_vector_type(2)));
typedef float f32x2v __attribute__((ext_vector_type(2)));
__device__ __forceinline__ unsigned pk2(float lo, float hi) { f32x2v v = {lo, hi}; return __builtin_bit_cast(unsigned, __builtin_convertvector(v, bf16x2)); }

__device__ __forceinline__ int otid() { int t = threadIdx.x; asm volatile("" : "+v"(t)); return t; }
constexpr int S = 2048, D = 640, DF = 321, H = 32, DH = 128, HD = 4096, M = 8, SEG = 32, NSEG = 64, L = 40;
__host__ __device__ inline int dmap(int ks, int gB, int jB) {
    int gA = 2 * (jB >> 3) + (gB >> 1), ja = 4 * (gB & 1) + 2 * ((jB >> 2) & 1) + (jB & 1), ph = (jB >> 1) & 1;
    return 64 * ks + 8 * (2 * gA + ph) + ja;
}

struct WS {
    const u16 *x, *wr, *wi, *mem0, *Wq, *Wk, *Wv, *Wo, *gw;
    u16 *out;
    unsigned char* wsb;
};
constexpr unsigned O_COMM = 64u << 10;
constexpr unsigned O_QXF = 2u << 20;
constexpr unsigned O_KXF = O_QXF + (unsigned)S * HD * 2;
constexpr unsigned O_UF = O_KXF + (unsigned)S * HD * 2 + (1u << 20);
constexpr unsigned O_PXF = O_UF + (unsigned)NSEG * 80 * 4096;
constexpr unsigned O_PMF = O_PXF + (unsigned)NSEG * 65536;
constexpr unsigned O_ABUF = O_PMF + (unsigned)NSEG * 16384;
constexpr unsigned O_FFT = O_ABUF + (unsigned)S * D * 2;
constexpr unsigned O_VWF = 84u << 20;
struct Pol { unsigned m0, m1, m2; };
__device__ __forceinline__ unsigned vw_base(const Pol& p, int s, int j) {
    const unsigned q = (unsigned)(s * 80 + j), r = (unsigned)__builtin_amdgcn_readfirstlane((int)(q >> 6));
    const unsigned m = r < 32 ? p.m0 : (r < 64 ? p.m1 : p.m2);
    return O_VWF + (r << 21) + ((q & 63u) << 15) + (((m >> (r & 31u)) & 1u) << 13);
}
__device__ __forceinline__ unsigned vw_off(unsigned o) { return ((o >> 13) << 14) | (o & 8191u); }
constexpr unsigned WS_SPAN = 250u << 20;
typedef __amdgpu_buffer_rsrc_t rsrc_t;
__device__ __forceinline__ rsrc_t ws_rsrc(const WS& ws) { return __builtin_amdgcn_make_buffer_rsrc(ws.wsb, 0, WS_SPAN, 0x00020000); }

#define GLDS16(src, dst) __builtin_amdgcn_global_load_lds((const unsigned*)(src), (LAS unsigned*)(dst), 16, 0, 0)
__device__ __forceinline__ void glds16_asm(const void* gsrc, LAS unsigned char* dst) {
    unsigned keep; const unsigned d = (unsigned)__builtin_amdgcn_readfirstlane((int)(unsigned)(size_t)dst);
    asm volatile("s_mov_b32 %0, m0\n\ts_mov_b32 m0, %2\n\ts_nop 0\n\tglobal_load_lds_dwordx4 %1, off\n\ts_mov_b32 m0, %0" : "=&s"(keep) : "v"(gsrc), "s"(d) : "memory");
}
__device__ __forceinline__ void glds16_sc1_asm(const void* gsrc, LAS unsigned char* dst) {
    unsigned keep; const unsigned d = (unsigned)__builtin_amdgcn_readfirstlane((int)(unsigned)(size_t)dst);
    asm volatile("s_mov_b32 %0, m0\n\ts_mov_b32 m0, %2\n\ts_nop 0\n\tglobal_load_lds_dwordx4 %1, off sc1\n\ts_mov_b32 m0, %0" : "=&s"(keep) : "v"(gsrc), "s"(d) : "memory");
}
__device__ __forceinline__ void st16(rsrc_t r, unsigned off, v4u v) { __builtin_amdgcn_raw_buffer_store_b128(v, r, off, 0, 16); }
__device__ __forceinline__ v4u ld16(rsrc_t r, unsigned off) { return __builtin_amdgcn_raw_buffer_load_b128(r, off, 0, 16); }
__device__ __forceinline__ v2u ld8(rsrc_t r, unsigned off) { return __builtin_amdgcn_raw_buffer_load_b64(r, off, 0, 16); }
__device__ __forceinline__ bf16x8 trfrag(LAS unsigned char* p0, LAS unsigned char* p1) {
    s16x4 lo = __builtin_amdgcn_ds_read_tr16_b64_v4i16((LAS s16x4*)p0);
    s16x4 hi = __builtin_amdgcn_ds_read_tr16_b64_v4i16((LAS s16x4*)p1);
    return __builtin_bit_cast(bf16x8, __builtin_shufflevector(lo, hi, 0, 1, 2, 3, 4, 5, 6, 7));
}
__device__ __forceinline__ v4u pack8(const f32x4& a, const f32x4& b) { v4u o = {pk2(a[0], a[1]), pk2(a[2], a[3]), pk2(b[0], b[1]), pk2(b[2], b[3])}; return o; }

__device__ __forceinline__ int wsw(int row) { return ((row & 3) | ((row >> 1) & 4)) << 1; }
__device__ __forceinline__ int wsw2(int row) { return ((row & 3) | (row & 4)) << 1; }

__device__ __forceinline__ float l16f(unsigned p) { return __uint_as_float(p << 16); }
__device__ __forceinline__ float h16f(unsigned p) { return __uint_as_float(p & 0xffff0000u); }
__device__ __forceinline__ float gmax(float x) {
    { auto t = __builtin_amdgcn_permlane16_swap(__float_as_uint(x), __float_as_uint(x), false, false); x = fmaxf(__uint_as_float(t[0]), __uint_as_float(t[1])); }
    { auto t = __builtin_amdgcn_permlane32_swap(__float_as_uint(x), __float_as_uint(x), false, false); x = fmaxf(__uint_as_float(t[0]), __uint_as_float(t[1])); }
    return x;
}
__device__ __forceinline__ float gsum(float x) {
    { auto t = __builtin_amdgcn_permlane16_swap(__float_as_uint(x), __float_as_uint(x), false, false); x = __uint_as_float(t[0]) + __uint_as_float(t[1]); }
    { auto t = __builtin_amdgcn_permlane32_swap(__float_as_uint(x), __float_as_uint(x), false, false); x = __uint_as_float(t[0]) + __uint_as_float(t[1]); }
    return x;
}

constexpr int NHEADCU = 32, NST2 = 40, NCHAIN = 64 + NST2;
constexpr int MBYTES = 10240, PBYTES = 32 * 640;
constexpr int MRING = 4, PRING = 4;
constexpr int COMM_M = 0, COMM_P = MRING * MBYTES, COMM_SM = COMM_P + PRING * PBYTES, COMM_SP = COMM_SM + 4 * 40 * 128  , COMM_SA = COMM_SP + 64 * 128 + 4096  , COMM_U = COMM_SA + 4 * 64 * 128  , UBYTES = 80 * 32 * 128, COMM_BYTES = COMM_U + 2 * UBYTES;
constexpr unsigned O_CTL = 0;
constexpr int C_ERR = 0, C_ROLE = 1, C_ARRIVE = 2, C_WID = 3, C_GHEAD = 4, C_QHEAD = 5, C_WVO = 6, C_WT = 7, C_DFT = 8, C_F1 = 9, C_F2 = 10, C_PROG = 11, C_PAR = 12, C_FIN = 13, C_INIT = 14, C_POLN = 15, C_POL = 26  ,
              C_XCD = 16, C_OVF = 24, C_F3 = 32, C_ATTA = 48, C_QK = 112, C_VW = 176, C_WVG = 240, C_NUM = 280;
constexpr unsigned CTL_END = O_CTL + C_NUM * 128;
constexpr int LDS_BYTES = 136 * 1024;
typedef __attribute__((address_space(1))) unsigned gu32;
__device__ __forceinline__ unsigned ctr_add(const WS& ws, int c) { return __hip_atomic_fetch_add((gu32*)(ws.wsb + O_CTL + c * 128), 1u, __ATOMIC_RELAXED, __HIP_MEMORY_SCOPE_AGENT) + 1u; }
__device__ __forceinline__ unsigned ctr_get(rsrc_t rc, int c) { return (unsigned)__builtin_amdgcn_readfirstlane(__builtin_amdgcn_raw_buffer_load_b32(rc, O_CTL + c * 128, 0, 16)) + 1u; }
__device__ __forceinline__ unsigned cxl(int off, int par) { return O_COMM + (unsigned)(((off >> 13) << 14) | (par << 13) | (off & 8191)); }

struct ChainCfg { int t1, t2, t3, pad, d0, pen, sd1, sd2, fl, fl2, pad2, pad3; };

#define VWNEAR 0
#define FINE_K 1
#define USL 5
#define CAUX 17
__device__ __forceinline__ v4u lc16(rsrc_t r, unsigned off) { return __builtin_amdgcn_raw_buffer_load_b128(r, off, 0, CAUX); }
__device__ __forceinline__ v2u lc8(rsrc_t r, unsigned off) { return __builtin_amdgcn_raw_buffer_load_b64(r, off, 0, CAUX); }
__device__ __forceinline__ unsigned vmax4(v4u v) { return max(max(v.x, v.y), max(v.z, v.w)); }
__device__ __forceinline__ float dpp_ror8(float x) { return __uint_as_float(__builtin_amdgcn_update_dpp(0, __float_as_uint(x), 0x128, 0xf, 0xf, false)); }
__device__ __forceinline__ float fmx(float a, float b) { return __builtin_amdgcn_fmed3f(a, b, __builtin_inff()); }
__device__ __forceinline__ float xsum(float x) { return gsum(x + dpp_ror8(x)); }
__device__ __forceinline__ unsigned long long now() { return __builtin_amdgcn_s_memtime(); }
__device__ __forceinline__ void set_err(rsrc_t rc, unsigned code) { __builtin_amdgcn_raw_buffer_store_b32(code, rc, O_CTL, 0, 16); }
__device__ __forceinline__ bool wait_ctr(rsrc_t rc, int c, unsigned n) {
    if (threadIdx.x < 64) {
        unsigned spins = 0;
        while (ctr_get(rc, c) < n) { __builtin_amdgcn_s_sleep(8); asm volatile("" ::: "memory"); if (++spins > (1u << 22)) { set_err(rc, 16 + c); break; } }
    }
    __syncthreads();
    return true;
}
__device__ __forceinline__ bool wait_prog(rsrc_t rc, int n) { return wait_ctr(rc, C_PROG, (unsigned)n); }
__device__ __forceinline__ void wave_wait_ctr(rsrc_t rc, int c, unsigned n) {
    unsigned spins = 0;
    while (ctr_get(rc, c) < n) { __builtin_amdgcn_s_sleep(4); asm volatile("" ::: "memory"); if (++spins > (1u << 22)) { set_err(rc, 16 + c); break; } }
}
__device__ __forceinline__ unsigned rt32() { return (unsigned)__builtin_amdgcn_s_memrealtime(); }
__device__ __forceinline__ void until_rt(unsigned t) { for (;;) { const int d = (int)(t - rt32()); if (d <= 0 || d > 100000) break; } }
__device__ __forceinline__ void until_rt_fine(unsigned t) {
    int d = (int)(t - rt32());
    if (d <= 4 || d > 100000) { until_rt(t); return; }
    do { d = (int)(t - rt32()); } while (d > 4);
    for (; d > FINE_K; --d) asm volatile("s_nop 15");
}
__device__ __forceinline__ unsigned stamp_now() { return rt32() & 0x7FFFFFFFu; }
__device__ __forceinline__ int wave_max_i32(int d) {
    d = max(d, __builtin_amdgcn_update_dpp(d, d, 0xB1, 0xf, 0xf, false));
    d = max(d, __builtin_amdgcn_update_dpp(d, d, 0x4E, 0xf, 0xf, false));
    d = max(d, __builtin_amdgcn_update_dpp(d, d, 0x124, 0xf, 0xf, false));
    d = max(d, __builtin_amdgcn_update_dpp(d, d, 0x128, 0xf, 0xf, false));
    { auto t = __builtin_amdgcn_permlane16_swap((unsigned)d, (unsigned)d, false, false); d = max((int)t[0], (int)t[1]); }
    { auto t = __builtin_amdgcn_permlane32_swap((unsigned)d, (unsigned)d, false, false); d = max((int)t[0], (int)t[1]); }
    return __builtin_amdgcn_readfirstlane(d);
}
__device__ __forceinline__ unsigned read_stamps(rsrc_t rc, int off, int par, int n, int lane, unsigned ref) {
    const unsigned o0 = cxl(off + (lane < n ? lane : 0) * 128, par), o1 = cxl(off + (lane + 64 < n ? lane + 64 : 0) * 128, par);
    for (unsigned spins = 0;; ++spins) {
        unsigned a = __builtin_amdgcn_raw_buffer_load_b32(rc, o0, 0, 16), b = a;
        if (n > 64) b = __builtin_amdgcn_raw_buffer_load_b32(rc, o1, 0, 16);
        if (__all(a != 0xFFFFFFFFu && b != 0xFFFFFFFFu)) {
            int d = max((int)((a - ref) << 1), (int)((b - ref) << 1)) >> 1;
            return (unsigned)wave_max_i32(d);
        }
        asm volatile("" ::: "memory");
        if (spins > (1u << 24)) { set_err(rc, 3); return 0; }
    }
}
__device__ __forceinline__ unsigned read_sched(rsrc_t rc, int s, int par, unsigned ref, int which) {
    for (unsigned spins = 0;; ++spins) {
        const unsigned v = (unsigned)__builtin_amdgcn_readfirstlane(__builtin_amdgcn_raw_buffer_load_b32(rc, cxl(COMM_SP + s * 128 + which * 4, par), 0, 16));
        if (v != 0xFFFFFFFFu) return ref + (unsigned)((int)((v - (ref & 0x7FFFFFFFu)) << 1) >> 1);
        asm volatile("" ::: "memory");
        if (spins > (1u << 22)) { set_err(rc, 4); return ref; }
    }
}
__device__ __forceinline__ unsigned eff_start(unsigned T, unsigned t, const ChainCfg& cfg) { return ((int)(T - t) >= cfg.d0) ? T : t + (unsigned)(cfg.d0 + cfg.pen); }
__device__ __forceinline__ void wait_target(volatile LAS unsigned* tg, unsigned tag) {
    unsigned spins = 0;
    while (tg[0] != tag) { __builtin_amdgcn_s_sleep(1); if (++spins > (1u << 24)) break; }
    until_rt_fine(tg[1]);
}
__device__ __forceinline__ void ensure_seg(rsrc_t rc, int cbase, int need, int& rdy) {
    if (need >= NSEG || need < rdy) return;
    const int far = min(need + 7, NSEG - 1);
    if (ctr_get(rc, cbase + far) >= 32) { rdy = far + 1; return; }
    wave_wait_ctr(rc, cbase + need, 32);
    rdy = need + 1;
}
__device__ __forceinline__ void load_M0(const WS& ws, LAS unsigned char* mlds, int tid) {
    for (int ch = tid; ch < 640; ch += 512) *(LAS v4u*)(mlds + ch * 16) = *(const v4u*)(ws.mem0 + (ch & 7) * D + (ch >> 3) * 8);
}
__device__ __forceinline__ int calib_parity(rsrc_t rc, LAS unsigned char* lds, int w, int lane) {
    LAS unsigned* res = (LAS unsigned*)lds;
    unsigned best[2] = {~0u, ~0u};
    for (int it = 0; it < 3; ++it)
        for (int q = 0; q < 2; ++q) {
            const int p = q ^ (w & 1);
            const unsigned off = cxl((w * 3 + it) * 8192 + 1024 * it, p);
            asm volatile("s_waitcnt vmcnt(0)" ::: "memory");
            const unsigned long long t0 = now();
            if (lane == 0) __builtin_amdgcn_raw_buffer_store_b32(~0u, rc, off, 0, 16);
            asm volatile("s_waitcnt vmcnt(0)" ::: "memory");
            const unsigned d = (unsigned)(now() - t0);
            if (p) best[1] = min(best[1], d); else best[0] = min(best[0], d);
        }
    if (lane == 0) { res[w * 2] = best[0]; res[w * 2 + 1] = best[1]; }
    __syncthreads();
    unsigned b0 = ~0u, b1 = ~0u;
    for (int i = 0; i < 8; ++i) { b0 = min(b0, res[i * 2]); b1 = min(b1, res[i * 2 + 1]); }
    return b1 < b0 ? 1 : 0;
}
__device__ __forceinline__ Pol wait_pol(rsrc_t rc) {
    wait_ctr(rc, C_POLN, NST2);
    Pol p; p.m0 = ctr_get(rc, C_POL) - 1u; p.m1 = ctr_get(rc, C_POL + 1) - 1u; p.m2 = ctr_get(rc, C_POL + 2) - 1u;
    return p;
}
__device__ __forceinline__ void calib_regions(const WS& ws, rsrc_t rc, LAS unsigned char* lds, int jp, int w, int lane) {
    LAS unsigned* res = (LAS unsigned*)lds;
    const unsigned base = O_VWF + ((unsigned)(2 * jp + (w >> 2)) << 21);
    unsigned b0 = ~0u, b1 = ~0u;
    for (int it = 0; it < 3; ++it)
        for (int q = 0; q < 2; ++q) {
            const int p = q ^ (w & 1);
            const unsigned off = base + (unsigned)(((w & 3) * 3 + it) * 5) * 16384u + (unsigned)p * 8192u + 1024u * it;
            asm volatile("s_waitcnt vmcnt(0)" ::: "memory");
            const unsigned long long t0 = now();
            if (lane == 0) __builtin_amdgcn_raw_buffer_store_b32(0u, rc, off, 0, 16);
            asm volatile("s_waitcnt vmcnt(0)" ::: "memory");
            const unsigned d = (unsigned)(now() - t0);
            if (p) b1 = min(b1, d); else b0 = min(b0, d);
        }
    if (lane == 0) { res[w * 2] = b0; res[w * 2 + 1] = b1; }
    __syncthreads();
    if (lane == 0 && (w & 3) == 0) {
        for (int i = 1; i < 4; ++i) { b0 = min(b0, res[(w + i) * 2]); b1 = min(b1, res[(w + i) * 2 + 1]); }
        const int r = 2 * jp + (w >> 2);
        if ((b1 < b0) != (VWNEAR != 0))
            __hip_atomic_fetch_and((gu32*)(ws.wsb + O_CTL + (C_POL + (r >> 5)) * 128), ~(1u << (r & 31)), __ATOMIC_RELAXED, __HIP_MEMORY_SCOPE_AGENT);
    }
    asm volatile("s_waitcnt vmcnt(0)" ::: "memory");
    __syncthreads();
    if (w == 0 && lane == 0) ctr_add(ws, C_POLN);
}
__device__ __forceinline__ int wait_init(rsrc_t rc) { wait_ctr(rc, C_INIT, NST2); return (int)ctr_get(rc, C_PAR) - 1; }

__device__ __forceinline__ bool sweep_M_head(rsrc_t rc, int slot, int par, LAS unsigned char* mlds, int w, int lane, volatile LAS unsigned* tg) {
    if (w == 5 || w == 6) return true;
    const bool two = w < 4;
    const int c0 = two ? w : (w == 4 ? 8 : 9), c1 = w + 4;
    const unsigned o0 = cxl(COMM_M + slot * MBYTES + c0 * 1024 + lane * 16, par);
    const unsigned o1 = cxl(COMM_M + slot * MBYTES + (c1 & 7) * 1024 + lane * 16, par);
    for (unsigned spins = 0;; ++spins) {
        v4u a = lc16(rc, o0);
        v4u b = {0, 0, 0, 0};
        if (two) b = lc16(rc, o1);
        *(LAS v4u*)(mlds + c0 * 1024 + lane * 16) = a;
        if (two) *(LAS v4u*)(mlds + c1 * 1024 + lane * 16) = b;
        unsigned mx = max(vmax4(a), vmax4(b));
        if (__all(mx != 0xFFFFFFFFu)) return true;
        asm volatile("" ::: "memory");
        if (spins > (1u << 22)) { set_err(rc, 1); return false; }
    }
}
__device__ __forceinline__ void head_role(const WS& ws, const ChainCfg& cfg, int h, LAS unsigned char* lds) {
    const int tid = otid(), lane = tid & 63, w = __builtin_amdgcn_readfirstlane(tid >> 6);
    const int g = lane >> 4, c = lane & 15, q4 = c >> 2, p4 = c & 3;
    const rsrc_t rc = ws_rsrc(ws);
    bf16x16 wq[10], wk[10];
    {
        auto stage = [&](int rd, int buf) {
#pragma unroll
            for (int i = 0; i < 8; ++i) {
                const int pc = w * 8 + i, mat = pc >> 5, row = (pc & 31) * 4 + (lane >> 4), ch = (lane & 15) ^ wsw2(row);
                const u16* W = mat ? ws.Wk : ws.Wq;
                GLDS16(W + (size_t)(rd * 128 + row) * HD + h * DH + ch * 8, lds + buf * 65536 + pc * 1024);
            }
        };
        stage(0, 0); stage(1, 1);
#pragma unroll
        for (int rd = 0; rd < 5; ++rd) {
            if (rd > 0 && rd + 1 < 5) { asm volatile("s_waitcnt lgkmcnt(0)" ::: "memory"); __builtin_amdgcn_s_barrier(); stage(rd + 1, (rd + 1) & 1); }
            if (rd + 1 < 5) { asm volatile("s_waitcnt vmcnt(8)" ::: "memory"); } else { asm volatile("s_waitcnt vmcnt(0)" ::: "memory"); }
            __builtin_amdgcn_s_barrier();
#pragma unroll
            for (int k2 = 0; k2 < 2; ++k2)
#pragma unroll
                for (int mat = 0; mat < 2; ++mat) {
                    v2u R[2][2];
#pragma unroll
                    for (int hi = 0; hi < 2; ++hi)
#pragma unroll
                        for (int ph = 0; ph < 2; ++ph) {
                            const int row = k2 * 64 + 16 * (2 * hi + (g >> 1)) + 8 * ph + 4 * (g & 1) + q4, ec = w * 2 + (p4 >> 1);
                            R[hi][ph] = __builtin_bit_cast(v2u, __builtin_amdgcn_ds_read_tr16_b64_v4i16((LAS s16x4*)(lds + (rd & 1) * 65536 + mat * 32768 + row * 256 + ((ec ^ wsw2(row)) << 4) + (p4 & 1) * 8)));
                        }
                    v8u t = {R[0][0].x, R[0][1].x, R[0][0].y, R[0][1].y, R[1][0].x, R[1][1].x, R[1][0].y, R[1][1].y};
                    if (mat == 0) wq[rd * 2 + k2] = __builtin_bit_cast(bf16x16, t); else wk[rd * 2 + k2] = __builtin_bit_cast(bf16x16, t);
                }
        }
        __syncthreads();
    }
    LAS unsigned char* mlds = lds; LAS unsigned char* T = lds + MBYTES; LAS unsigned char* kq = lds + 32768;
    const int par = wait_init(rc);
    wait_ctr(rc, C_QK + 0, 32);
    const int idx = (lane & 8) ? 0xEEEE : 0x4444;
    const float scale = 0.08838834764831845f, l2e = 1.4426950408889634f;
    { v4u t = ld16(rc, O_KXF + (unsigned)(0 * 32 + h) * 8192 + tid * 16), t2 = ld16(rc, O_QXF + (unsigned)(0 * 32 + h) * 8192 + tid * 16); *(LAS v4u*)(kq + tid * 16) = t; *(LAS v4u*)(kq + 8192 + tid * 16) = t2; }
    auto dma_kq = [&](int seg) {
        const unsigned char* src = ws.wsb + (w == 5 ? O_KXF : O_QXF) + (size_t)(seg * 32 + h) * 8192 + lane * 16;
        LAS unsigned char* dst = kq + (seg % 3) * 16384 + (w - 5) * 8192;
#pragma unroll
        for (int i = 0; i < 8; ++i) glds16_sc1_asm(src + i * 1024, dst + i * 1024);
    };
    int rdy = 0; unsigned TMc = 0;
    volatile LAS unsigned* tg = (volatile LAS unsigned*)(lds + MBYTES + 4096);
    if (tid == 0) { tg[0] = 0; tg[2] = 0; }
    for (int s = 0; s < NSEG; ++s) {
        LAS unsigned char* kxl = kq + (s % 3) * 16384; LAS unsigned char* qxl = kxl + 8192;
        unsigned pso = cxl(COMM_P + (s & (PRING - 1)) * PBYTES + h * 640 + (g * 8 + (c & 7)) * 16 + (c >> 3) * 8, par);
        unsigned pso2 = cxl(COMM_P + (s & (PRING - 1)) * PBYTES + h * 640 + 512 + (c & 7) * 16 + (g & 1) * 8 + (c >> 3) * 4, par);
        asm volatile("" : "+v"(pso), "+v"(pso2));
        if (s == 0) load_M0(ws, mlds, tid);
        else {
            if (w != 5 && w != 6) wait_target(tg, (unsigned)s);
            if (!sweep_M_head(rc, s & (MRING - 1), par, mlds, w, lane, tg)) return;
        }
        asm volatile("" ::: "memory"); __builtin_amdgcn_s_waitcnt(0xC07F); __builtin_amdgcn_s_barrier(); asm volatile("" ::: "memory");

        v4u af[10];
#pragma unroll
        for (int ks = 0; ks < 10; ++ks) af[ks] = *(LAS v4u*)(mlds + ks * 1024 + lane * 16);
        __builtin_amdgcn_sched_barrier(0);
        f32x4 dq = {0, 0, 0, 0}, dk = {0, 0, 0, 0};
#pragma unroll
        for (int ks = 0; ks < 10; ++ks) {
            dq = __builtin_amdgcn_smfmac_f32_16x16x64_bf16(__builtin_bit_cast(bf16x8, af[ks]), wq[ks], dq, idx, 0, 0);
            dk = __builtin_amdgcn_smfmac_f32_16x16x64_bf16(__builtin_bit_cast(bf16x8, af[ks]), wk[ks], dk, idx, 0, 0);
        }
        float v[4];
#pragma unroll
        for (int r = 0; r < 4; ++r) {
            auto sw = __builtin_amdgcn_permlane32_swap(__float_as_uint(dq[r]), __float_as_uint(dk[r]), false, false);
            v[r] = __uint_as_float(sw[0]) + __uint_as_float(sw[1]);
        }
        v2u pk = {pk2(v[0], v[1]), pk2(v[2], v[3])};
        *(LAS v2u*)(T + (16 * w + c) * 32 + g * 8) = pk;
        asm volatile("" ::: "memory"); __builtin_amdgcn_s_waitcnt(0xC07F); __builtin_amdgcn_s_barrier(); asm volatile("" ::: "memory");
        if (w == 0 && s < NSEG - 1) {
            __builtin_amdgcn_s_setprio(3);
            f32x4 smm = {0, 0, 0, 0}, s0 = {0, 0, 0, 0}, s1 = {0, 0, 0, 0};
            s16x4 lo[4], hi[4]; v4u kx[8];
#pragma unroll
            for (int k4 = 0; k4 < 4; ++k4) {
                lo[k4] = __builtin_amdgcn_ds_read_tr16_b64_v4i16((LAS s16x4*)(T + (32 * k4 + 4 * g + q4) * 32 + p4 * 8));
                hi[k4] = __builtin_amdgcn_ds_read_tr16_b64_v4i16((LAS s16x4*)(T + (32 * k4 + 16 + 4 * g + q4) * 32 + p4 * 8));
                kx[k4] = *(LAS v4u*)(kxl + k4 * 1024 + lane * 16);
                kx[4 + k4] = *(LAS v4u*)(kxl + (4 + k4) * 1024 + lane * 16);
            }
            __builtin_amdgcn_sched_barrier(0);
#pragma unroll
            for (int k4 = 0; k4 < 4; ++k4) {
                bf16x8 f = __builtin_bit_cast(bf16x8, __builtin_shufflevector(lo[k4], hi[k4], 0, 1, 2, 3, 4, 5, 6, 7));
                smm = __builtin_amdgcn_mfma_f32_16x16x32_bf16(f, f, smm, 0, 0, 0);
                s0 = __builtin_amdgcn_mfma_f32_16x16x32_bf16(__builtin_bit_cast(bf16x8, kx[k4]), f, s0, 0, 0, 0);
                s1 = __builtin_amdgcn_mfma_f32_16x16x32_bf16(__builtin_bit_cast(bf16x8, kx[4 + k4]), f, s1, 0, 0, 0);
            }
            float x[4];
#pragma unroll
            for (int r = 0; r < 4; ++r) x[r] = __uint_as_float(__builtin_amdgcn_update_dpp(__float_as_uint(s0[r]), __float_as_uint(s1[r]), 0x118, 0xf, 0xc, false));
            unsigned y0 = __builtin_amdgcn_update_dpp(__float_as_uint(smm[0]), __float_as_uint(smm[2]), 0x118, 0xf, 0xc, false);
            unsigned y1 = __builtin_amdgcn_update_dpp(__float_as_uint(smm[1]), __float_as_uint(smm[3]), 0x118, 0xf, 0xc, false);
            float xm = __uint_as_float(__builtin_amdgcn_permlane32_swap(y0, y1, false, false)[1]);
            float mr = fmx(fmx(fmx(x[0], x[1]), fmx(x[2], x[3])), xm);
            mr = fmx(mr, dpp_ror8(mr));
            { auto t = __builtin_amdgcn_permlane16_swap(__float_as_uint(mr), __float_as_uint(mr), false, false); mr = fmx(__uint_as_float(t[0]), __uint_as_float(t[1])); }
            { auto t = __builtin_amdgcn_permlane32_swap(__float_as_uint(mr), __float_as_uint(mr), false, false); mr = fmx(__uint_as_float(t[0]), __uint_as_float(t[1])); }
            unsigned a01 = pk2(x[0], x[1]), a23 = pk2(x[2], x[3]), am = pk2(xm, mr);
            unsigned b01 = pk2(l16f(a01) * scale, h16f(a01) * scale), b23 = pk2(l16f(a23) * scale, h16f(a23) * scale), bm = pk2(l16f(am) * scale, h16f(am) * scale);
            const float nm = -h16f(bm) * l2e;
            float e0 = __builtin_amdgcn_exp2f(__builtin_fmaf(l16f(b01), l2e, nm)), e1 = __builtin_amdgcn_exp2f(__builtin_fmaf(h16f(b01), l2e, nm));
            float e2 = __builtin_amdgcn_exp2f(__builtin_fmaf(l16f(b23), l2e, nm)), e3 = __builtin_amdgcn_exp2f(__builtin_fmaf(h16f(b23), l2e, nm));
            float em = __builtin_amdgcn_exp2f(__builtin_fmaf(l16f(bm), l2e, nm));
            auto sw = __builtin_amdgcn_permlane32_swap(__float_as_uint(em), __float_as_uint(em), false, false);
            float sum = xsum(((e0 + e1) + (e2 + e3)) + em);
            const float inv = __builtin_amdgcn_rcpf(sum);
            v2u px = {min(pk2(e0 * inv, e1 * inv), 0xFFFFFFFEu), min(pk2(e2 * inv, e3 * inv), 0xFFFFFFFEu)};
            __builtin_amdgcn_raw_buffer_store_b64(px, rc, pso, 0, 16);
            unsigned pm = min(pk2(__uint_as_float(sw[0]) * inv, __uint_as_float(sw[1]) * inv), 0xFFFFFFFEu);
            if (g < 2) __builtin_amdgcn_raw_buffer_store_b32(pm, rc, pso2, 0, 16);
            if (lane == 0) {
                __builtin_amdgcn_raw_buffer_store_b32(stamp_now(), rc, cxl(COMM_SA + ((s & 3) * 64 + h) * 128, par), 0, 16);
                __builtin_amdgcn_raw_buffer_store_b32(~0u, rc, cxl(COMM_SA + (((s + 2) & 3) * 64 + h) * 128, par), 0, 16);
            }
            __builtin_amdgcn_s_setprio(0);
            const int pz = COMM_P + ((s + 2) & (PRING - 1)) * PBYTES + h * 640;
            if (lane < 40) { v4u ones = {~0u, ~0u, ~0u, ~0u}; st16(rc, cxl(pz + lane * 16, par), ones); }
        } else if (w == 3 && s < NSEG - 1) {
            const unsigned ref = rt32();
            unsigned T = ref + cfg.pad;
            if (s > 0) T = read_sched(rc, s, par, ref, 1);
            if (lane == 0) { tg[1] = T; asm volatile("s_waitcnt lgkmcnt(0)" ::: "memory"); tg[0] = (unsigned)(s + 1); }
        } else if (w == 5 || w == 6) {
            const int tt = w - 5;
            if (s > 0) { asm volatile("s_waitcnt vmcnt(0)" ::: "memory"); if (lane == 0) ctr_add(ws, C_ATTA + s - 1); }
            f32x4 sc[3];
#pragma unroll
            for (int kt = 0; kt < 3; ++kt) sc[kt] = (f32x4){0, 0, 0, 0};
#pragma unroll
            for (int k4 = 0; k4 < 4; ++k4) {
                bf16x8 f = trfrag(T + (32 * k4 + 4 * g + q4) * 32 + p4 * 8, T + (32 * k4 + 16 + 4 * g + q4) * 32 + p4 * 8);
                bf16x8 k0 = __builtin_bit_cast(bf16x8, *(LAS v4u*)(kxl + k4 * 1024 + lane * 16));
                bf16x8 k1 = __builtin_bit_cast(bf16x8, *(LAS v4u*)(kxl + (4 + k4) * 1024 + lane * 16));
                bf16x8 bq = __builtin_bit_cast(bf16x8, *(LAS v4u*)(qxl + (tt * 4 + k4) * 1024 + lane * 16));
                sc[0] = __builtin_amdgcn_mfma_f32_16x16x32_bf16(f, bq, sc[0], 0, 0, 0);
                sc[1] = __builtin_amdgcn_mfma_f32_16x16x32_bf16(k0, bq, sc[1], 0, 0, 0);
                sc[2] = __builtin_amdgcn_mfma_f32_16x16x32_bf16(k1, bq, sc[2], 0, 0, 0);
            }
            const float NEG = rbf(-1e9f);
            float u[12];
#pragma unroll
            for (int kt = 0; kt < 3; ++kt)
#pragma unroll
                for (int r = 0; r < 4; r += 2) {
                    unsigned a_ = pk2(sc[kt][r], sc[kt][r + 1]);
                    unsigned b_ = pk2(l16f(a_) * scale, h16f(a_) * scale);
                    u[kt * 4 + r] = l16f(b_); u[kt * 4 + r + 1] = h16f(b_);
                }
            const int qq = 16 * tt + c;
#pragma unroll
            for (int r = 0; r < 4; ++r) {
                if (g < 2) u[r] = -INFINITY;
                if (4 * g + r > qq) u[4 + r] = NEG;
                if (16 + 4 * g + r > qq) u[8 + r] = NEG;
            }
            float mx = u[0];
#pragma unroll
            for (int i2 = 1; i2 < 12; ++i2) mx = fmaxf(mx, u[i2]);
            mx = gmax(mx);
            const float nm = -mx * l2e; float sum = 0;
#pragma unroll
            for (int i2 = 0; i2 < 12; ++i2) { u[i2] = __builtin_amdgcn_exp2f(__builtin_fmaf(u[i2], l2e, nm)); sum += u[i2]; }
            sum = gsum(sum);
            const float inv = __builtin_amdgcn_rcpf(sum);
            v4u px = {pk2(u[4] * inv, u[5] * inv), pk2(u[6] * inv, u[7] * inv), pk2(u[8] * inv, u[9] * inv), pk2(u[10] * inv, u[11] * inv)};
            st16(rc, O_PXF + (unsigned)(((s * 32 + h) * 2 + tt) * 64 + lane) * 16u, px);
            unsigned m0 = pk2(u[0] * inv, u[1] * inv), m1 = pk2(u[2] * inv, u[3] * inv);
            auto x0 = __builtin_amdgcn_permlane16_swap(m0, m0, false, false), x1 = __builtin_amdgcn_permlane16_swap(m1, m1, false, false);
            v4u pm4 = {x0[0], x1[0], x0[1], x1[1]};
            if (g == 2) st16(rc, O_PMF + (unsigned)(((s * 32 + h) * 2 + tt) * 16 + c) * 16u, pm4);
            if (s == NSEG - 1) { asm volatile("s_waitcnt vmcnt(0)" ::: "memory"); if (lane == 0) ctr_add(ws, C_ATTA + s); }
            if (s == 0) { ensure_seg(rc, C_QK, 1, rdy); dma_kq(1); asm volatile("s_waitcnt vmcnt(0)" ::: "memory"); }
            if (s + 2 < NSEG) { ensure_seg(rc, C_QK, s + 2, rdy); dma_kq(s + 2); }
        }
        if (h == 0 && tid == 448 && s > 0) __builtin_amdgcn_raw_buffer_store_b32((unsigned)(s - 1), rc, O_CTL + C_PROG * 128, 0, 16);
    }
    asm volatile("s_waitcnt vmcnt(0)" ::: "memory");
    __syncthreads();
    if (tid == 0 && h == 0) { if (ctr_add(ws, C_FIN) == 32) __builtin_amdgcn_raw_buffer_store_b32((unsigned)(NSEG + 1), rc, O_CTL + C_PROG * 128, 0, 16); }
}

__device__ __forceinline__ void u_role(const WS& ws, const ChainCfg& cfg, int h, LAS unsigned char* lds) {
    const int tid = otid(), lane = tid & 63, w = __builtin_amdgcn_readfirstlane(tid >> 6);
    const int g = lane >> 4, c = lane & 15, q4 = c >> 2, p4 = c & 3;
    const int w4 = w & 3, mat = w >> 2;
    const rsrc_t rc = ws_rsrc(ws);
    bf16x16 wt[2][10];
    {
        auto stage = [&](int rd, int buf) {
#pragma unroll
            for (int i = 0; i < 8; ++i) {
                const int pc = w * 8 + i, m = pc >> 5, row = (pc & 31) * 4 + (lane >> 4), ch = (lane & 15) ^ wsw2(row);
                if (m == 0) GLDS16(ws.Wv + (size_t)(rd * 128 + row) * HD + h * DH + ch * 8, lds + buf * 65536 + pc * 1024);
                else GLDS16(ws.Wo + (size_t)(h * DH + row) * D + rd * 128 + ch * 8, lds + buf * 65536 + pc * 1024);
            }
        };
        stage(0, 0); stage(1, 1);
#pragma unroll
        for (int rd = 0; rd < 5; ++rd) {
            if (rd > 0 && rd + 1 < 5) { asm volatile("s_waitcnt lgkmcnt(0)" ::: "memory"); __builtin_amdgcn_s_barrier(); stage(rd + 1, (rd + 1) & 1); }
            if (rd + 1 < 5) { asm volatile("s_waitcnt vmcnt(8)" ::: "memory"); } else { asm volatile("s_waitcnt vmcnt(0)" ::: "memory"); }
            __builtin_amdgcn_s_barrier();
#pragma unroll
            for (int k2 = 0; k2 < 2; ++k2)
#pragma unroll
                for (int i = 0; i < 2; ++i) {
                    v2u R[2][2];
#pragma unroll
                    for (int hi = 0; hi < 2; ++hi)
#pragma unroll
                        for (int ph = 0; ph < 2; ++ph) {
                            const int row = k2 * 64 + 16 * (2 * hi + (g >> 1)) + 8 * ph + 4 * (g & 1) + q4, ec = (2 * w4 + i) * 2 + (p4 >> 1);
                            R[hi][ph] = __builtin_bit_cast(v2u, __builtin_amdgcn_ds_read_tr16_b64_v4i16((LAS s16x4*)(lds + (rd & 1) * 65536 + mat * 32768 + row * 256 + ((ec ^ wsw2(row)) << 4) + (p4 & 1) * 8)));
                        }
                    v8u t = {R[0][0].x, R[0][1].x, R[0][0].y, R[0][1].y, R[1][0].x, R[1][1].x, R[1][0].y, R[1][1].y};
                    wt[i][rd * 2 + k2] = __builtin_bit_cast(bf16x16, t);
                }
        }
        __syncthreads();
    }
    LAS unsigned char* mlds = lds; LAS unsigned char* T = lds + MBYTES;
    const int par = wait_init(rc);
    const int idx = (lane & 8) ? 0xEEEE : 0x4444;
    volatile LAS unsigned* tg = (volatile LAS unsigned*)(lds + MBYTES + 4096);
    if (tid == 0) tg[0] = 0;
    for (int s = 0; s < NSEG; ++s) {
        if (s == 0) load_M0(ws, mlds, tid);
        else if (mat == 0) {
            wait_target(tg, (unsigned)s);
            const int mb = COMM_M + (s & (MRING - 1)) * MBYTES + lane * 16;
            const unsigned o0 = cxl(mb + w4 * 1024, par), o1 = cxl(mb + (w4 + 4) * 1024, par), o2 = cxl(mb + (8 + (w4 & 1)) * 1024, par);
            for (unsigned spins = 0;; ++spins) {
                v4u a_ = lc16(rc, o0), b_ = lc16(rc, o1), c_ = {0, 0, 0, 0};
                if (w4 < 2) c_ = lc16(rc, o2);
                *(LAS v4u*)(mlds + w4 * 1024 + lane * 16) = a_; *(LAS v4u*)(mlds + (w4 + 4) * 1024 + lane * 16) = b_;
                if (w4 < 2) *(LAS v4u*)(mlds + (8 + w4) * 1024 + lane * 16) = c_;
                if (__all(max(max(vmax4(a_), vmax4(b_)), vmax4(c_)) != 0xFFFFFFFFu)) break;
                asm volatile("" ::: "memory");
                if (spins > (1u << 22)) { set_err(rc, 1); return; }
            }
        }
        __syncthreads();
        if (mat == 0) {
            v4u af[10];
#pragma unroll
            for (int ks = 0; ks < 10; ++ks) af[ks] = *(LAS v4u*)(mlds + ks * 1024 + lane * 16);
            __builtin_amdgcn_sched_barrier(0);
            f32x4 d0 = {0, 0, 0, 0}, d1 = {0, 0, 0, 0};
#pragma unroll
            for (int ks = 0; ks < 10; ++ks) {
                d0 = __builtin_amdgcn_smfmac_f32_16x16x64_bf16(__builtin_bit_cast(bf16x8, af[ks]), wt[0][ks], d0, idx, 0, 0);
                d1 = __builtin_amdgcn_smfmac_f32_16x16x64_bf16(__builtin_bit_cast(bf16x8, af[ks]), wt[1][ks], d1, idx, 0, 0);
            }
            float v[4];
#pragma unroll
            for (int r = 0; r < 4; ++r) {
                auto sw = __builtin_amdgcn_permlane32_swap(__float_as_uint(d0[r]), __float_as_uint(d1[r]), false, false);
                v[r] = __uint_as_float(sw[0]) + __uint_as_float(sw[1]);
            }
            v2u pk = {pk2(v[0], v[1]), pk2(v[2], v[3])};
            *(LAS v2u*)(T + (32 * w4 + 16 * (g >> 1) + c) * 16 + (g & 1) * 8) = pk;
            __syncthreads();
            if (w == 3 && s < NSEG - 1) {
                const unsigned ref = rt32();
                unsigned T2 = ref + cfg.pad + 40;
                if (s > 0) T2 = read_sched(rc, s, par, ref, 1);
                if (lane == 0) { tg[1] = T2; asm volatile("s_waitcnt lgkmcnt(0)" ::: "memory"); tg[0] = (unsigned)(s + 1); }
            }
        } else {
            unsigned uo[5];
            { const int ub = COMM_U + (s & 1) * UBYTES + h * 128 + (g & 1) * 64 + (c & 7) * 8;
#pragma unroll
              for (int t = 0; t < 5; ++t) uo[t] = cxl(ub + ((8 * t + 2 * w4 + (g >> 1)) * 2 + (c >> 3)) * 4096, par);
              asm volatile("" : "+v"(uo[0]), "+v"(uo[1]), "+v"(uo[2]), "+v"(uo[3]), "+v"(uo[4])); }
            asm volatile("s_waitcnt vmcnt(0)" ::: "memory");
            __syncthreads();
            v2u ok[5];
            bf16x8 av[2];
#pragma unroll
            for (int k2 = 0; k2 < 2; ++k2) {
                LAS unsigned char* p = T + (64 * k2 + 16 * g + 8 * (p4 >> 1) + q4) * 16 + (p4 & 1) * 8;
                av[k2] = trfrag(p, p + 64);
            }
#pragma unroll
            for (int t = 0; t < 5; ++t) {
                f32x4 u0 = {0, 0, 0, 0}, u1 = {0, 0, 0, 0};
                u0 = __builtin_amdgcn_smfmac_f32_16x16x64_bf16(av[0], wt[0][2 * t], u0, idx, 0, 0);
                u1 = __builtin_amdgcn_smfmac_f32_16x16x64_bf16(av[0], wt[1][2 * t], u1, idx, 0, 0);
                u0 = __builtin_amdgcn_smfmac_f32_16x16x64_bf16(av[1], wt[0][2 * t + 1], u0, idx, 0, 0);
                u1 = __builtin_amdgcn_smfmac_f32_16x16x64_bf16(av[1], wt[1][2 * t + 1], u1, idx, 0, 0);
                float z[4];
#pragma unroll
                for (int r = 0; r < 4; ++r) {
                    auto sw = __builtin_amdgcn_permlane32_swap(__float_as_uint(u0[r]), __float_as_uint(u1[r]), false, false);
                    z[r] = __uint_as_float(sw[0]) + __uint_as_float(sw[1]);
                }
                v2u o = {min(pk2(z[0], z[1]), 0xFFFFFFFEu), min(pk2(z[2], z[3]), 0xFFFFFFFEu)};
                __builtin_amdgcn_raw_buffer_store_b64(o, rc, uo[t], 0, 16);
                ok[t] = o;
            }
            if (w == 7 && lane == 0) {
                __builtin_amdgcn_raw_buffer_store_b32((rt32() + USL * 3 - 4) & 0x7FFFFFFFu, rc, cxl(COMM_SA + ((s & 3) * 64 + 32 + h) * 128, par), 0, 16);
                __builtin_amdgcn_raw_buffer_store_b32(~0u, rc, cxl(COMM_SA + (((s + 2) & 3) * 64 + 32 + h) * 128, par), 0, 16);
            }
            until_rt(rt32() + cfg.sd1);
#pragma unroll
            for (int t = 0; t < 5; ++t)
                __builtin_amdgcn_raw_buffer_store_b64(ok[t], rc, O_UF + (unsigned)(s * 80 + (8 * t + 2 * w4 + (g >> 1)) * 2 + (c >> 3)) * 4096u + h * 128 + (g & 1) * 64 + (c & 7) * 8, 0, 16);
            if (s > 0) {
                const int pb = COMM_U + ((s + 1) & 1) * UBYTES + h * 128 + (lane & 7) * 16;
                v4u ones = {~0u, ~0u, ~0u, ~0u};
                st16(rc, cxl(pb + (w4 * 20 + (lane >> 3)) * 4096, par), ones);
                st16(rc, cxl(pb + (w4 * 20 + 8 + (lane >> 3)) * 4096, par), ones);
                if (lane < 32) st16(rc, cxl(pb + (w4 * 20 + 16 + (lane >> 3)) * 4096, par), ones);
            }
        }
    }
    asm volatile("s_waitcnt vmcnt(0)" ::: "memory");
    __syncthreads();
    if (tid == 0) { if (ctr_add(ws, C_FIN) == 32) __builtin_amdgcn_raw_buffer_store_b32((unsigned)(NSEG + 1), rc, O_CTL + C_PROG * 128, 0, 16); }
}

__device__ __forceinline__ void st2_role(const WS& ws, const ChainCfg& cfg, int jp, LAS unsigned char* lds) {
    const int tid = otid(), lane = tid & 63, w = __builtin_amdgcn_readfirstlane(tid >> 6);
    const int g = lane >> 4, c = lane & 15;
    LAS unsigned char* red = lds;
    const rsrc_t rc = ws_rsrc(ws);
    const int hh = (lane >> 3) & 1, i = lane & 7;
    calib_regions(ws, rc, lds, jp, w, lane);
    wait_ctr(rc, C_PAR, 1);
    {
        const int p0 = (int)ctr_get(rc, C_PAR) - 1;
        v4u ones = {~0u, ~0u, ~0u, ~0u};
        for (int q = jp * 512 + tid; q < COMM_BYTES / 16; q += NST2 * 512) st16(rc, cxl(q * 16, p0), ones);
        asm volatile("s_waitcnt vmcnt(0)" ::: "memory");
        __syncthreads();
        if (tid == 0) ctr_add(ws, C_INIT);
    }
    const int par = wait_init(rc);
    const Pol pol = wait_pol(rc);
    int rdy = 0;
    volatile LAS unsigned* tg = (volatile LAS unsigned*)(lds + 8192);
    if (tid == 0) tg[0] = 0;
    unsigned tpub = rt32();
    LAS unsigned char* vwl = lds + 16384;
    auto dma_vw = [&](int seg) {
        if (w == 3 || seg >= NSEG - 1) return;
        const int wi = w > 3 ? w - 1 : w;
#pragma unroll
        for (int q = 0; q < 5; ++q) {
            const int pc = wi + 7 * q;
            if (pc < 32) glds16_sc1_asm(ws.wsb + vw_base(pol, seg, 2 * jp + (pc >> 4)) + vw_off((pc & 15) * 1024) + lane * 16, vwl + (seg % 3) * 32768 + pc * 1024);
        }
    };
    wait_ctr(rc, C_VW, 32);
    dma_vw(0);
    asm volatile("s_waitcnt vmcnt(0)" ::: "memory");
    wait_ctr(rc, C_VW + 1, 32);
    dma_vw(1);
    for (int s = 0; s < NSEG - 1; ++s) {
        v4u vw[2][2];
#pragma unroll
        for (int jj = 0; jj < 2; ++jj) { vw[jj][0] = *(LAS v4u*)(vwl + (s % 3) * 32768 + (jj * 16 + 2 * w) * 1024 + lane * 16); vw[jj][1] = *(LAS v4u*)(vwl + (s % 3) * 32768 + (jj * 16 + 2 * w + 1) * 1024 + lane * 16); }
        if (w == 3) ensure_seg(rc, C_VW, s + 2, rdy);
        if (w == 3 && s > 0) {
            until_rt(tpub + cfg.t2);
            const unsigned T = read_sched(rc, s, par, tpub, 0);
            if (lane == 0) { tg[1] = T; asm volatile("s_waitcnt lgkmcnt(0)" ::: "memory"); tg[0] = (unsigned)s; }
        }
        const int ou = COMM_U + (s & 1) * UBYTES + (2 * jp * 32 + 4 * w + 2 * (g >> 1) + hh) * 128 + (g & 1) * 64 + i * 8;
        const int pb = COMM_P + (s & (PRING - 1)) * PBYTES;
        unsigned ou0 = cxl(ou, par), ou1 = cxl(ou + 4096, par);
        unsigned ox0 = cxl(pb + (4 * w + hh) * 640 + (g * 8 + i) * 16, par);
        unsigned ox1 = cxl(pb + (4 * w + 2 + hh) * 640 + (g * 8 + i) * 16, par);
        unsigned om = cxl(pb + (4 * w + 2 * (g >> 1) + hh) * 640 + 512 + i * 16 + (g & 1) * 8, par);
        unsigned omw = cxl(COMM_M + ((s + 1) & (MRING - 1)) * MBYTES + (2 * jp + (w & 1)) * 128 + (lane & 15) * 8 + (g >> 1) * 4, par);
        asm volatile("" : "+v"(omw), "+v"(ou0), "+v"(ou1), "+v"(ox0), "+v"(ox1), "+v"(om));
        unsigned e = 0;
        if (s > 0) {
            unsigned spins = 0;
            while (tg[0] != (unsigned)s) { __builtin_amdgcn_s_sleep(1); if (++spins > (1u << 24)) break; }
            e = tg[1];
            until_rt_fine(e - USL * 3);
        }
        v2u uf0, uf1;
        {
            f32x4 a0, a1;
            for (unsigned spins = 0;; ++spins) {
                uf0 = lc8(rc, ou0); uf1 = lc8(rc, ou1);
                if (USL > 0 && spins == 0) { asm volatile("" ::: "memory"); __builtin_amdgcn_s_sleep(USL); asm volatile("" ::: "memory"); }
                v4u p0 = lc16(rc, ox0), p1 = lc16(rc, ox1); v2u pm = lc8(rc, om);
                f32x4 b0 = {0, 0, 0, 0}, b1 = {0, 0, 0, 0};
                a0 = (f32x4){0, 0, 0, 0}; a1 = (f32x4){0, 0, 0, 0};
                a0 = __builtin_amdgcn_mfma_f32_16x16x32_bf16(__builtin_bit_cast(bf16x8, vw[0][0]), __builtin_bit_cast(bf16x8, p0), a0, 0, 0, 0);
                a1 = __builtin_amdgcn_mfma_f32_16x16x32_bf16(__builtin_bit_cast(bf16x8, vw[1][0]), __builtin_bit_cast(bf16x8, p0), a1, 0, 0, 0);
                a0 = __builtin_amdgcn_mfma_f32_16x16x32_bf16(__builtin_bit_cast(bf16x8, vw[0][1]), __builtin_bit_cast(bf16x8, p1), a0, 0, 0, 0);
                a1 = __builtin_amdgcn_mfma_f32_16x16x32_bf16(__builtin_bit_cast(bf16x8, vw[1][1]), __builtin_bit_cast(bf16x8, p1), a1, 0, 0, 0);
                b0 = __builtin_amdgcn_mfma_f32_16x16x16bf16_1k(__builtin_bit_cast(s16x4, uf0), __builtin_bit_cast(s16x4, pm), b0, 0, 0, 0);
                b1 = __builtin_amdgcn_mfma_f32_16x16x16bf16_1k(__builtin_bit_cast(s16x4, uf1), __builtin_bit_cast(s16x4, pm), b1, 0, 0, 0);
                __builtin_amdgcn_sched_barrier(0);
                unsigned mx = max(max(max(vmax4(p0), vmax4(p1)), max(pm.x, pm.y)), max(max(uf0.x, uf0.y), max(uf1.x, uf1.y)));
                a0 += b0; a1 += b1;
                if (__all(mx != 0xFFFFFFFFu)) break;
                asm volatile("" ::: "memory");
                if (spins > (1u << 22)) { set_err(rc, 2); return; }
            }
            if (hh == (g >> 1)) {
                *(LAS f32x4*)(red + ((w * 2 + hh) * 16 + i * 2 + (g & 1)) * 16) = a0;
                *(LAS f32x4*)(red + 4096 + ((w * 2 + hh) * 16 + i * 2 + (g & 1)) * 16) = a1;
            }
            __syncthreads();
            if (w == 3) tpub = rt32();
            if (w < 2) {
                LAS unsigned char* rd = red + w * 4096;
                f32x4 t0 = *(LAS f32x4*)(rd + (g * 16 + c) * 16);
                f32x4 t1 = *(LAS f32x4*)(rd + ((g + 4) * 16 + c) * 16);
                f32x4 t2 = *(LAS f32x4*)(rd + ((g + 8) * 16 + c) * 16);
                f32x4 t3 = *(LAS f32x4*)(rd + ((g + 12) * 16 + c) * 16);
                f32x4 t = (t0 + t1) + (t2 + t3);
                auto x0 = __builtin_amdgcn_permlane16_swap(__float_as_uint(t[0]), __float_as_uint(t[1]), false, false);
                auto x1 = __builtin_amdgcn_permlane16_swap(__float_as_uint(t[2]), __float_as_uint(t[3]), false, false);
                float u0 = __uint_as_float(x0[0]) + __uint_as_float(x0[1]);
                float u1 = __uint_as_float(x1[0]) + __uint_as_float(x1[1]);
                auto x2 = __builtin_amdgcn_permlane32_swap(__float_as_uint(u0), __float_as_uint(u1), false, false);
                float z = __uint_as_float(x2[0]) + __uint_as_float(x2[1]);
                auto y0 = __builtin_amdgcn_permlane16_swap(__float_as_uint(z), __float_as_uint(z), false, false);
                unsigned pr = min(pk2(__uint_as_float(y0[0]), __uint_as_float(y0[1])), 0xFFFFFFFEu);
                if ((g & 1) == 0) __builtin_amdgcn_raw_buffer_store_b32(pr, rc, omw, 0, 16);
                if (lane == 0 && w == 1) {
                    __builtin_amdgcn_raw_buffer_store_b32(stamp_now(), rc, cxl(COMM_SM + (((s + 1) & 3) * 40 + jp) * 128, par), 0, 16);
                    __builtin_amdgcn_raw_buffer_store_b32(~0u, rc, cxl(COMM_SM + (((s + 3) & 3) * 40 + jp) * 128, par), 0, 16);
                }
                if (lane < 8) { v4u ones = {~0u, ~0u, ~0u, ~0u}; st16(rc, cxl(COMM_M + ((s + 3) & (MRING - 1)) * MBYTES + (2 * jp + w) * 128 + lane * 16, par), ones); }
            }
        }
        if (w != 3) until_rt(rt32() + cfg.sd2);
        dma_vw(s + 2);
    }
    asm volatile("s_waitcnt vmcnt(0)" ::: "memory");
}

__device__ __forceinline__ void sched_role(const WS& ws, const ChainCfg& cfg, LAS unsigned char* lds) {
    const int tid = otid(), lane = tid & 63;
    const rsrc_t rc = ws_rsrc(ws);
    const int par = calib_parity(rc, lds, __builtin_amdgcn_readfirstlane(tid >> 6), lane);
    if (tid >= 64) return;
    if (lane == 0) __builtin_amdgcn_raw_buffer_store_b32((unsigned)par, rc, O_CTL + C_PAR * 128, 0, 16);
    wave_wait_ctr(rc, C_INIT, NST2);
    unsigned TM = rt32() - 100000u, TP = TM;
    int a16 = cfg.t3 << 4, b16 = cfg.t1 << 4;
    bool pclean = false;
    for (int s = 1; s < NSEG - 1; ++s) {
        if (s > 1) until_rt(TM + 10);
        unsigned ref = rt32();
        const unsigned tM = ref + read_stamps(rc, COMM_SM + (s & 3) * 5120, par, 40, lane, ref & 0x7FFFFFFFu);
        const unsigned e = eff_start(TM, tM, cfg);
        const bool mclean = e == TM;
        if (pclean && cfg.pad3 <= 0) { const int m = min(max((int)(tM - TP) << 4, b16 - 160), b16 + 160); b16 += (m - b16) >> 2; }
        TP = e + (unsigned)((a16 + 8) >> 4) + (unsigned)cfg.fl;
        const unsigned TMn = TP + (unsigned)((b16 + 8) >> 4) + (unsigned)cfg.pad2;
        if (lane == 0) { v2u o = {TP & 0x7FFFFFFFu, TMn & 0x7FFFFFFFu}; __builtin_amdgcn_raw_buffer_store_b64(o, rc, cxl(COMM_SP + s * 128, par), 0, 16); }
        TM = TMn;
        if (s < NSEG - 1 && cfg.pad3 < 2) {
            until_rt(TP + 30);
            ref = rt32();
            const unsigned tP = ref + read_stamps(rc, COMM_SA + (s & 3) * 8192, par, 64, lane, ref & 0x7FFFFFFFu);
            pclean = (int)(TP - tP) >= -2;
            if (mclean && cfg.pad3 <= 0) { const int m = min(max((int)(tP - e) << 4, a16 - 160), a16 + 160); a16 += (m - a16) >> 2; }
        }
    }
}

constexpr unsigned O_FF = O_FFT;
constexpr unsigned O_GF = O_FF + 40 * 20 * 2 * 1024;
constexpr unsigned O_WT = O_GF + 40 * 20 * 2 * 1024;
constexpr unsigned O_XT = O_WT + 324 * 2048 * 4;
constexpr unsigned O_YH = O_XT + 640 * 2048 * 4;
constexpr unsigned O_YL = O_YH + 640 * 2048 * 2;
constexpr unsigned O_FFT_END = O_YL + 640 * 2048 * 2;

#define split2(a, b, hi, lo) do { unsigned h_ = pk2(a, b); hi = h_; lo = pk2((a) - l16f(h_), (b) - h16f(h_)); } while (0)

__device__ __forceinline__ void wave_dftmat(const WS& ws, int item) {
    const int lane = otid() & 63, g = lane >> 4, c = lane & 15;
    const int which = item / 800, tile = (item % 800) / 20, k = item % 20;
    const rsrc_t rw = ws_rsrc(ws);
    float v[8];
#pragma unroll
    for (int j = 0; j < 8; ++j) {
        int n, d;
        if (which == 0) { n = 16 * tile + c; d = 32 * k + 8 * g + j; }
        else { n = 32 * k + 8 * g + j; d = 16 * tile + c; }
        const bool im = n > 320; const int f = im ? n - 320 : n;
        const int idx = (f * d) % D;
        float sn, cs; sincospif((float)idx * (2.0f / D), &sn, &cs);
        float val = im ? -sn : cs;
        if (which == 1 && f != 0 && f != 320) val *= 2.0f;
        v[j] = val;
    }
    v4u hi, lo;
    split2(v[0], v[1], hi.x, lo.x); split2(v[2], v[3], hi.y, lo.y); split2(v[4], v[5], hi.z, lo.z); split2(v[6], v[7], hi.w, lo.w);
    const unsigned o = (which ? O_GF : O_FF) + (unsigned)((tile * 20 + k) * 2) * 1024 + lane * 16;
    st16(rw, o, hi); st16(rw, o + 1024, lo);
}

struct cf { float x, y; };
__device__ __forceinline__ cf cadd(cf a, cf b) { return {a.x + b.x, a.y + b.y}; }
__device__ __forceinline__ cf csub(cf a, cf b) { return {a.x - b.x, a.y - b.y}; }
__device__ __forceinline__ cf cmul(cf a, cf b) { return {a.x * b.x - a.y * b.y, a.x * b.y + a.y * b.x}; }
template <int DIR> __device__ __forceinline__ cf mulI(cf a) { return DIR < 0 ? cf{a.y, -a.x} : cf{-a.y, a.x}; }
template <int DIR> __device__ __forceinline__ void dft4(cf& a, cf& b, cf& c, cf& d) {
    cf s0 = cadd(a, c), s1 = csub(a, c), s2 = cadd(b, d), s3 = mulI<DIR>(csub(b, d));
    a = cadd(s0, s2); c = csub(s0, s2); b = cadd(s1, s3); d = csub(s1, s3);
}
template <int DIR> __device__ __forceinline__ cf tw16(cf a, int k) {
    const float C1 = 0.92387953251128674f, S1 = 0.38268343236508977f, R = 0.70710678118654752f;
    const float cs[10] = {1.f, C1, R, S1, 0.f, -S1, -R, -C1, -1.f, -C1}, sn[10] = {0.f, S1, R, C1, 1.f, C1, R, S1, 0.f, -S1};
    if (k == 0) return a;
    const float c = cs[k], s = DIR * sn[k];
    return {a.x * c - a.y * s, a.x * s + a.y * c};
}
template <int DIR> __device__ __forceinline__ void dft16(cf (&v)[16]) {
#pragma unroll
    for (int i = 0; i < 4; ++i) dft4<DIR>(v[i], v[i + 4], v[i + 8], v[i + 12]);
#pragma unroll
    for (int i = 1; i < 4; ++i)
#pragma unroll
        for (int k1 = 1; k1 < 4; ++k1) v[i + 4 * k1] = tw16<DIR>(v[i + 4 * k1], i * k1);
#pragma unroll
    for (int k1 = 0; k1 < 4; ++k1) dft4<DIR>(v[4 * k1], v[4 * k1 + 1], v[4 * k1 + 2], v[4 * k1 + 3]);
#pragma unroll
    for (int a = 0; a < 4; ++a)
#pragma unroll
        for (int b = a + 1; b < 4; ++b) { cf t = v[4 * a + b]; v[4 * a + b] = v[4 * b + a]; v[4 * b + a] = t; }
}
template <int DIR> __device__ __forceinline__ void dft8(cf* v) {
    dft4<DIR>(v[0], v[2], v[4], v[6]); dft4<DIR>(v[1], v[3], v[5], v[7]);
    v[3] = tw16<DIR>(v[3], 2); v[5] = tw16<DIR>(v[5], 4); v[7] = tw16<DIR>(v[7], 6);
    cf o[8];
#pragma unroll
    for (int k1 = 0; k1 < 4; ++k1) { o[k1] = cadd(v[2 * k1], v[2 * k1 + 1]); o[k1 + 4] = csub(v[2 * k1], v[2 * k1 + 1]); }
#pragma unroll
    for (int i = 0; i < 8; ++i) v[i] = o[i];
}
__device__ __forceinline__ int fpad(int i) { return i + (i >> 4); }
__device__ __forceinline__ void task_f2(const WS& ws, int fg, LAS unsigned char* lds, bool make_table) {
    const int tid = otid(), col = tid >> 7, t = tid & 127;
    const rsrc_t rw = ws_rsrc(ws);
    LAS f32x2v* tw = (LAS f32x2v*)lds;
    LAS f32x2v* buf = (LAS f32x2v*)(lds + 16384) + col * 2176;
    if (make_table) {
        for (int i = tid; i < 2048; i += 512) { float sn, cs; sincospif((float)i * (1.0f / 1024.0f), &sn, &cs); tw[i] = (f32x2v){cs, sn}; }
    }
    const int f = fg * 4 + col;
    const bool valid = f < DF, hasim = valid && f != 0 && f != 320;
    const int fr = valid ? f : 0;
    cf v[16];
#pragma unroll
    for (int s2 = 0; s2 < 16; ++s2) {
        v[s2].x = __uint_as_float(__builtin_amdgcn_raw_buffer_load_b32(rw, O_XT + (unsigned)(fr * 2048 + s2 * 128 + t) * 4u, 0, 16));
        v[s2].y = hasim ? __uint_as_float(__builtin_amdgcn_raw_buffer_load_b32(rw, O_XT + (unsigned)((320 + fr) * 2048 + s2 * 128 + t) * 4u, 0, 16)) : 0.f;
    }
    v4u wv[4];
#pragma unroll
    for (int i = 0; i < 4; ++i) wv[i] = ld16(rw, O_WT + (unsigned)fr * 8192 + t * 64 + i * 16);
    __syncthreads();
    dft16<-1>(v);
#pragma unroll
    for (int k0 = 1; k0 < 16; ++k0) { f32x2v w = tw[k0 * t]; v[k0] = cmul(v[k0], cf{w.x, -w.y}); }
#pragma unroll
    for (int k0 = 0; k0 < 16; ++k0) buf[fpad(k0 * 128 + t)] = (f32x2v){v[k0].x, v[k0].y};
    __syncthreads();
    { const int k0 = t >> 3, s0 = t & 7;
#pragma unroll
      for (int s1 = 0; s1 < 16; ++s1) { f32x2v q = buf[fpad(k0 * 128 + s1 * 8 + s0)]; v[s1] = cf{q.x, q.y}; }
      dft16<-1>(v);
#pragma unroll
      for (int k1 = 1; k1 < 16; ++k1) { f32x2v w = tw[k1 * s0 * 16]; v[k1] = cmul(v[k1], cf{w.x, -w.y}); }
      __syncthreads();
#pragma unroll
      for (int k1 = 0; k1 < 16; ++k1) buf[fpad((k0 * 16 + k1) * 8 + s0)] = (f32x2v){v[k1].x, v[k1].y};
    }
    __syncthreads();
#pragma unroll
    for (int hf = 0; hf < 2; ++hf) {
#pragma unroll
        for (int s0 = 0; s0 < 8; ++s0) { f32x2v q = buf[fpad((t + 128 * hf) * 8 + s0)]; v[hf * 8 + s0] = cf{q.x, q.y}; }
        dft8<-1>(v + hf * 8);
#pragma unroll
        for (int k2 = 0; k2 < 8; ++k2) { const unsigned ww = wv[hf * 2 + (k2 >> 2)][k2 & 3]; v[hf * 8 + k2] = cmul(v[hf * 8 + k2], cf{l16f(ww), h16f(ww)}); }
        dft8<1>(v + hf * 8);
#pragma unroll
        for (int s0 = 0; s0 < 8; ++s0) buf[fpad((t + 128 * hf) * 8 + s0)] = (f32x2v){v[hf * 8 + s0].x, v[hf * 8 + s0].y};
    }
    __syncthreads();
    { const int k0 = t >> 3, s0 = t & 7;
#pragma unroll
      for (int k1 = 0; k1 < 16; ++k1) { f32x2v q = buf[fpad((k0 * 16 + k1) * 8 + s0)]; v[k1] = cf{q.x, q.y}; }
#pragma unroll
      for (int k1 = 1; k1 < 16; ++k1) { f32x2v w = tw[k1 * s0 * 16]; v[k1] = cmul(v[k1], cf{w.x, w.y}); }
      dft16<1>(v);
      __syncthreads();
#pragma unroll
      for (int s1 = 0; s1 < 16; ++s1) buf[fpad(k0 * 128 + s1 * 8 + s0)] = (f32x2v){v[s1].x, v[s1].y};
    }
    __syncthreads();
#pragma unroll
    for (int k0 = 0; k0 < 16; ++k0) { f32x2v q = buf[fpad(k0 * 128 + t)]; v[k0] = cf{q.x, q.y}; }
#pragma unroll
    for (int k0 = 1; k0 < 16; ++k0) { f32x2v w = tw[k0 * t]; v[k0] = cmul(v[k0], cf{w.x, w.y}); }
    dft16<1>(v);
    __syncthreads();
#pragma unroll
    for (int s2 = 0; s2 < 16; ++s2) buf[fpad(s2 * 128 + t)] = (f32x2v){v[s2].x, v[s2].y};
    __syncthreads();
#pragma unroll
    for (int hf = 0; hf < 2; ++hf) {
        const int s0 = 8 * (t + 128 * hf);
        f32x2v q[8];
#pragma unroll
        for (int i = 0; i < 8; ++i) q[i] = buf[fpad(s0 + i)];
        v4u rh, rl, ih, il;
        split2(q[0].x, q[1].x, rh.x, rl.x); split2(q[2].x, q[3].x, rh.y, rl.y); split2(q[4].x, q[5].x, rh.z, rl.z); split2(q[6].x, q[7].x, rh.w, rl.w);
        split2(q[0].y, q[1].y, ih.x, il.x); split2(q[2].y, q[3].y, ih.y, il.y); split2(q[4].y, q[5].y, ih.z, il.z); split2(q[6].y, q[7].y, ih.w, il.w);
        const unsigned yo = (unsigned)(((s0 >> 5) * 640 + f) * 32 + (s0 & 31)) * 2u;
        if (valid) { st16(rw, O_YH + yo, rh); st16(rw, O_YL + yo, rl); }
        if (hasim) { st16(rw, O_YH + yo + 320 * 64, ih); st16(rw, O_YL + yo + 320 * 64, il); }
    }
    __syncthreads();
}

__device__ __forceinline__ void stage_x(const WS& ws, int s, LAS unsigned char* dst, int w, int lane) {
#pragma unroll
    for (int i = 0; i < 5; ++i) {
        const int pc = w * 5 + i, q = pc * 64 + lane, row = q / 80, slot = q - row * 80;
        glds16_asm(ws.x + (size_t)(s * 32 + row) * D + (slot ^ (row & 15)) * 8, dst + pc * 1024);
    }
}
__device__ __forceinline__ bf16x8 xfrag(LAS unsigned char* xt, int tt, int k, int c, int g) {
    return __builtin_bit_cast(bf16x8, *(LAS v4u*)(xt + (c * 1280 + (((4 * (k & 3) + g) ^ c) << 4)) + (tt * 20480 + (k >> 2) * 256)));
}

#define XPIPE(xt, BODY, NB) do { \
    bf16x8 xb_[2][2 * NB]; \
    _Pragma("unroll") for (int q_ = 0; q_ < NB; ++q_) { xb_[0][2 * q_] = xfrag(xt, 0, q_, c, g); xb_[0][2 * q_ + 1] = xfrag(xt, 1, q_, c, g); } \
    __builtin_amdgcn_sched_barrier(0); \
    _Pragma("unroll") for (int b_ = 0; b_ < 20 / NB; ++b_) { \
        BODY(NB * b_, xb_[b_ & 1][0], xb_[b_ & 1][1]); \
        __builtin_amdgcn_sched_barrier(0); \
        if (b_ + 1 < 20 / NB) { _Pragma("unroll") for (int q_ = 0; q_ < NB; ++q_) { xb_[(b_ + 1) & 1][2 * q_] = xfrag(xt, 0, NB * b_ + NB + q_, c, g); xb_[(b_ + 1) & 1][2 * q_ + 1] = xfrag(xt, 1, NB * b_ + NB + q_, c, g); } } \
        __builtin_amdgcn_sched_barrier(0); \
        _Pragma("unroll") for (int q_ = 1; q_ < NB; ++q_) { BODY(NB * b_ + q_, xb_[b_ & 1][2 * q_], xb_[b_ & 1][2 * q_ + 1]); } \
        __builtin_amdgcn_sched_barrier(0); \
    } } while (0)

__device__ __forceinline__ void qk_role(const WS& ws, int h, LAS unsigned char* lds) {
    const int tid = otid(), lane = tid & 63, w = __builtin_amdgcn_readfirstlane(tid >> 6);
    const int g = lane >> 4, c = lane & 15, q4 = c >> 2, p4 = c & 3;
    const rsrc_t rc = ws_rsrc(ws);
    const int mat = w >> 2, k4 = w & 3;
    bf16x8 aw[2][20];
    {
        auto stage = [&](int rd, int buf) {
#pragma unroll
            for (int i = 0; i < 8; ++i) {
                const int pc = w * 8 + i, m = pc >> 5, row = (pc & 31) * 4 + (lane >> 4), ch = (lane & 15) ^ wsw(row);
                const u16* W = m ? ws.Wk : ws.Wq;
                GLDS16(W + (size_t)(rd * 128 + row) * HD + h * DH + ch * 8, lds + buf * 65536 + pc * 1024);
            }
        };
        stage(0, 0); stage(1, 1);
#pragma unroll
        for (int rd = 0; rd < 5; ++rd) {
            if (rd > 0 && rd + 1 < 5) { asm volatile("s_waitcnt lgkmcnt(0)" ::: "memory"); __builtin_amdgcn_s_barrier(); stage(rd + 1, (rd + 1) & 1); }
            if (rd + 1 < 5) { asm volatile("s_waitcnt vmcnt(8)" ::: "memory"); } else { asm volatile("s_waitcnt vmcnt(0)" ::: "memory"); }
            __builtin_amdgcn_s_barrier();
#pragma unroll
            for (int kk = 0; kk < 4; ++kk)
#pragma unroll
                for (int i = 0; i < 2; ++i) {
                    const int r0 = kk * 32 + 8 * g + q4, r1 = r0 + 4, ec = (2 * k4 + i) * 2 + (p4 >> 1);
                    LAS unsigned char* m = lds + (rd & 1) * 65536 + mat * 32768 + (p4 & 1) * 8;
                    aw[i][rd * 4 + kk] = trfrag(m + r0 * 256 + ((ec ^ wsw(r0)) << 4), m + r1 * 256 + ((ec ^ wsw(r1)) << 4));
                }
        }
        __syncthreads();
    }
    stage_x(ws, 0, lds, w, lane);
    const unsigned obase = (mat == 0 ? O_QXF : O_KXF) + (unsigned)(h * 8 + k4) * 1024 + lane * 16;
    for (int s = 0; s < NSEG; ++s) {
        if (s < 3) { asm volatile("s_waitcnt vmcnt(0) lgkmcnt(0)" ::: "memory"); } else { asm volatile("s_waitcnt vmcnt(2) lgkmcnt(0)" ::: "memory"); }
        __builtin_amdgcn_s_barrier();
        if (tid == 0) { if (s > 0 && s < 3) ctr_add(ws, C_QK + s - 1); else if (s > 3) ctr_add(ws, C_QK + s - 2); }
        if (s + 1 < NSEG) stage_x(ws, s + 1, lds + ((s + 1) & 1) * 40960, w, lane);
        LAS unsigned char* xt = lds + (s & 1) * 40960;
        f32x4 acc[2][2];
#pragma unroll
        for (int tt = 0; tt < 2; ++tt) { acc[0][tt] = (f32x4){0, 0, 0, 0}; acc[1][tt] = (f32x4){0, 0, 0, 0}; }
#define QK_BODY(k, x0, x1) do { \
            acc[0][0] = __builtin_amdgcn_mfma_f32_16x16x32_bf16(aw[0][k], x0, acc[0][0], 0, 0, 0); acc[1][0] = __builtin_amdgcn_mfma_f32_16x16x32_bf16(aw[1][k], x0, acc[1][0], 0, 0, 0); \
            acc[0][1] = __builtin_amdgcn_mfma_f32_16x16x32_bf16(aw[0][k], x1, acc[0][1], 0, 0, 0); acc[1][1] = __builtin_amdgcn_mfma_f32_16x16x32_bf16(aw[1][k], x1, acc[1][1], 0, 0, 0); } while (0)
        XPIPE(xt, QK_BODY, 2);
#pragma unroll
        for (int tt = 0; tt < 2; ++tt) st16(rc, obase + (unsigned)s * 262144u + tt * 4096, pack8(acc[0][tt], acc[1][tt]));
    }
    asm volatile("s_waitcnt vmcnt(0)" ::: "memory");
    __syncthreads();
    if (tid == 0) { ctr_add(ws, C_QK + NSEG - 2); ctr_add(ws, C_QK + NSEG - 1); }
}

__device__ __forceinline__ void vo_role(const WS& ws, int h, LAS unsigned char* lds) {
    const int tid = otid(), lane = tid & 63, w = __builtin_amdgcn_readfirstlane(tid >> 6);
    const int g = lane >> 4, c = lane & 15, q4 = c >> 2, p4 = c & 3;
    const int p = w & 3, kh = w >> 2;
    const rsrc_t rc = ws_rsrc(ws);
    bf16x8 av[2][10], bo[5][4];
    {
        auto stage = [&](int rd, int buf) {
#pragma unroll
            for (int i = 0; i < 8; ++i) {
                const int pc = w * 8 + i, m = pc >> 5, row = (pc & 31) * 4 + (lane >> 4);
                if (m == 0) GLDS16(ws.Wv + (size_t)(rd * 128 + row) * HD + h * DH + ((lane & 15) ^ wsw(row)) * 8, lds + buf * 65536 + pc * 1024);
                else GLDS16(ws.Wo + (size_t)(h * DH + row) * D + rd * 128 + ((lane & 15) ^ wsw2(row)) * 8, lds + buf * 65536 + pc * 1024);
            }
        };
        stage(0, 0); stage(1, 1);
#pragma unroll
        for (int rd = 0; rd < 5; ++rd) {
            if (rd > 0 && rd + 1 < 5) { asm volatile("s_waitcnt lgkmcnt(0)" ::: "memory"); __builtin_amdgcn_s_barrier(); stage(rd + 1, (rd + 1) & 1); }
            if (rd + 1 < 5) { asm volatile("s_waitcnt vmcnt(8)" ::: "memory"); } else { asm volatile("s_waitcnt vmcnt(0)" ::: "memory"); }
            __builtin_amdgcn_s_barrier();
            LAS unsigned char* m = lds + (rd & 1) * 65536 + (p4 & 1) * 8;
#pragma unroll
            for (int k2 = 0; k2 < 2; ++k2)
#pragma unroll
                for (int i = 0; i < 2; ++i) {
                    const int r0 = (2 * k2 + kh) * 32 + 8 * g + q4, r1 = r0 + 4, ec = (2 * p + i) * 2 + (p4 >> 1);
                    av[i][rd * 2 + k2] = trfrag(m + r0 * 256 + ((ec ^ wsw(r0)) << 4), m + r1 * 256 + ((ec ^ wsw(r1)) << 4));
                }
            const int ec = w * 2 + (p4 >> 1);
#pragma unroll
            for (int kk = 0; kk < 4; ++kk) {
                const int c0 = kk * 32 + 4 * g + q4, c1 = c0 + 16;
                bo[rd][kk] = trfrag(m + 32768 + c0 * 256 + ((ec ^ wsw2(c0)) << 4), m + 32768 + c1 * 256 + ((ec ^ wsw2(c1)) << 4));
            }
        }
        __syncthreads();
    }
    const Pol pol = wait_pol(rc);
    const unsigned vwo = vw_off((unsigned)((h >> 1) * 64 + ((h & 1) * 8 + (c & 7) + 16 * g)) * 16u);
    LAS unsigned char* vf = lds + 81920;
    LAS unsigned char* xch = lds + 90112;
    const int xo0 = c * 1280 + (((4 * kh + g) ^ c) << 4), xo1 = c * 1280 + (((8 + 4 * kh + g) ^ c) << 4);
    stage_x(ws, 0, lds, w, lane);
#pragma unroll 1
    for (int s = 0; s < NSEG; ++s) {
        if (s <= 1) { asm volatile("s_waitcnt vmcnt(0) lgkmcnt(0)" ::: "memory"); } else { asm volatile("s_waitcnt vmcnt(5) lgkmcnt(0)" ::: "memory"); }
        __builtin_amdgcn_s_barrier();
        if (tid == 0 && s == 1) ctr_add(ws, C_VW);
        if (s + 1 < NSEG) stage_x(ws, s + 1, lds + ((s + 1) & 1) * 40960, w, lane);
        LAS unsigned char* xt = lds + (s & 1) * 40960;
        f32x4 acc[2][2];
#pragma unroll
        for (int i = 0; i < 2; ++i) { acc[i][0] = (f32x4){0, 0, 0, 0}; acc[i][1] = (f32x4){0, 0, 0, 0}; }
#define XF(m_, tt_) __builtin_bit_cast(bf16x8, *(LAS v4u*)(xt + (((m_) & 1) ? xo1 : xo0) + (tt_) * 20480 + ((m_) >> 1) * 256))
        bf16x8 xb[2][4];
#pragma unroll
        for (int q = 0; q < 2; ++q) { xb[0][2 * q] = XF(q, 0); xb[0][2 * q + 1] = XF(q, 1); }
#pragma unroll
        for (int bt = 0; bt < 5; ++bt) {
            if (bt + 1 < 5) {
#pragma unroll
                for (int q = 0; q < 2; ++q) { xb[(bt + 1) & 1][2 * q] = XF(2 * bt + 2 + q, 0); xb[(bt + 1) & 1][2 * q + 1] = XF(2 * bt + 2 + q, 1); }
            }
            __builtin_amdgcn_sched_barrier(0);
#pragma unroll
            for (int q = 0; q < 2; ++q)
#pragma unroll
                for (int i = 0; i < 2; ++i) {
                    acc[i][0] = __builtin_amdgcn_mfma_f32_16x16x32_bf16(av[i][2 * bt + q], xb[bt & 1][2 * q], acc[i][0], 0, 0, 0);
                    acc[i][1] = __builtin_amdgcn_mfma_f32_16x16x32_bf16(av[i][2 * bt + q], xb[bt & 1][2 * q + 1], acc[i][1], 0, 0, 0);
                }
            __builtin_amdgcn_sched_barrier(0);
        }
        *(LAS f32x4*)(xch + (w * 2 + 0) * 1024 + lane * 16) = kh ? acc[0][0] : acc[0][1];
        *(LAS f32x4*)(xch + (w * 2 + 1) * 1024 + lane * 16) = kh ? acc[1][0] : acc[1][1];
        if (s + 1 < NSEG) { asm volatile("s_waitcnt vmcnt(5) lgkmcnt(0)" ::: "memory"); } else { asm volatile("s_waitcnt vmcnt(0) lgkmcnt(0)" ::: "memory"); }
        __builtin_amdgcn_s_barrier();
        if (tid == 0 && s > 1) ctr_add(ws, C_VW + s - 1);
        {
            f32x4 o0 = *(LAS f32x4*)(xch + ((w ^ 4) * 2 + 0) * 1024 + lane * 16), o1 = *(LAS f32x4*)(xch + ((w ^ 4) * 2 + 1) * 1024 + lane * 16);
            f32x4 m0 = kh ? acc[0][1] : acc[0][0], m1 = kh ? acc[1][1] : acc[1][0];
            *(LAS v4u*)(vf + (kh * 4 + p) * 1024 + lane * 16) = pack8(m0 + o0, m1 + o1);
        }
        asm volatile("s_waitcnt lgkmcnt(0)" ::: "memory");
        __builtin_amdgcn_s_barrier();
        bf16x8 v0[4], v1[4];
#pragma unroll
        for (int kk = 0; kk < 4; ++kk) { v0[kk] = __builtin_bit_cast(bf16x8, *(LAS v4u*)(vf + kk * 1024 + lane * 16)); v1[kk] = __builtin_bit_cast(bf16x8, *(LAS v4u*)(vf + (4 + kk) * 1024 + lane * 16)); }
#pragma unroll
        for (int cc = 0; cc < 5; ++cc) {
            f32x4 d0 = {0, 0, 0, 0}, d1 = {0, 0, 0, 0};
#pragma unroll
            for (int kk = 0; kk < 4; ++kk) {
                d0 = __builtin_amdgcn_mfma_f32_16x16x32_bf16(v0[kk], bo[cc][kk], d0, 0, 0, 0);
                d1 = __builtin_amdgcn_mfma_f32_16x16x32_bf16(v1[kk], bo[cc][kk], d1, 0, 0, 0);
            }
            const int j = (cc * 128 + w * 16 + c) >> 3;
            st16(rc, vw_base(pol, s, j) + vwo, pack8(d0, d1));
        }
    }
    asm volatile("s_waitcnt vmcnt(0)" ::: "memory");
    __syncthreads();
    if (tid == 0) ctr_add(ws, C_VW + NSEG - 1);
}

__device__ __forceinline__ void task_f1s(const WS& ws, int cb, int s0, int ns, LAS unsigned char* lds) {
    const int tid = otid(), lane = tid & 63, w = __builtin_amdgcn_readfirstlane(tid >> 6);
    const int g = lane >> 4, c = lane & 15;
    const rsrc_t rw = ws_rsrc(ws);
    stage_x(ws, s0, lds, w, lane);
    v4u bh[20], bl[20];
    const unsigned fo = O_FF + (unsigned)(cb * 8 + w) * 40960 + lane * 16;
#pragma unroll
    for (int k = 0; k < 20; ++k) { bh[k] = ld16(rw, fo + k * 2048); bl[k] = ld16(rw, fo + k * 2048 + 1024); }
    const int col = cb * 128 + w * 16 + c;
    for (int i = 0; i < ns; ++i) {
        const int s = s0 + i;
        if (i == 0) { asm volatile("s_waitcnt vmcnt(0) lgkmcnt(0)" ::: "memory"); } else { asm volatile("s_waitcnt vmcnt(2) lgkmcnt(0)" ::: "memory"); }
        __builtin_amdgcn_s_barrier();
        if (i + 1 < ns) stage_x(ws, s + 1, lds + ((i + 1) & 1) * 40960, w, lane);
        LAS unsigned char* xt = lds + (i & 1) * 40960;
        f32x4 a0 = {0, 0, 0, 0}, a1 = {0, 0, 0, 0};
#define F1_BODY(k, x0, x1) do { \
            a0 = __builtin_amdgcn_mfma_f32_16x16x32_bf16(x0, __builtin_bit_cast(bf16x8, bl[k]), a0, 0, 0, 0); a1 = __builtin_amdgcn_mfma_f32_16x16x32_bf16(x1, __builtin_bit_cast(bf16x8, bl[k]), a1, 0, 0, 0); \
            a0 = __builtin_amdgcn_mfma_f32_16x16x32_bf16(x0, __builtin_bit_cast(bf16x8, bh[k]), a0, 0, 0, 0); a1 = __builtin_amdgcn_mfma_f32_16x16x32_bf16(x1, __builtin_bit_cast(bf16x8, bh[k]), a1, 0, 0, 0); } while (0)
        XPIPE(xt, F1_BODY, 2);
        st16(rw, O_XT + (unsigned)(col * 2048 + s * 32 + 4 * g) * 4u, __builtin_bit_cast(v4u, a0));
        st16(rw, O_XT + (unsigned)(col * 2048 + s * 32 + 16 + 4 * g) * 4u, __builtin_bit_cast(v4u, a1));
    }
}

__device__ __forceinline__ void task_f3s(const WS& ws, int db, int s0, int ns, LAS unsigned char* lds) {
    const int tid = otid(), lane = tid & 63, w = __builtin_amdgcn_readfirstlane(tid >> 6);
    const int g = lane >> 4, c = lane & 15, q4 = c >> 2, p4 = c & 3;
    const rsrc_t rw = ws_rsrc(ws);
    v4u gh[20], gl[20];
    const unsigned go = O_GF + (unsigned)(db * 8 + w) * 40960 + lane * 16;
#pragma unroll
    for (int k = 0; k < 20; ++k) { gh[k] = ld16(rw, go + k * 2048); gl[k] = ld16(rw, go + k * 2048 + 1024); }
    wait_ctr(rw, C_F2, 81);
    v4u st[5];
    const int pl = tid >> 8, t8 = tid & 255;
    const unsigned gsrc = (pl ? O_YL : O_YH) + t8 * 16;
    LAS unsigned char* ldst = lds + pl * 20480 + (t8 >> 2) * 64 + (((t8 & 3) ^ (((t8 >> 5) & 1) << 1)) << 4);
    auto gload = [&](int s, int hf) {
#pragma unroll
        for (int i = 0; i < 5; ++i) st[i] = ld16(rw, gsrc + (unsigned)s * 40960u + hf * 20480 + i * 4096);
    };
    auto lwrite = [&](int buf) {
#pragma unroll
        for (int i = 0; i < 5; ++i) *(LAS v4u*)(ldst + buf * 40960 + i * 4096) = st[i];
    };
    gload(s0, 0); lwrite(0); gload(s0, 1);
    __syncthreads();
    const float sc = 1.0f / (2048.0f * 640.0f);
    for (int i = 0; i < ns; ++i) {
        const int s = s0 + i;
        f32x4 a0 = {0, 0, 0, 0}, a1 = {0, 0, 0, 0};
#pragma unroll
        for (int hf = 0; hf < 2; ++hf) {
            LAS unsigned char* base = lds + hf * 40960;
#pragma unroll
            for (int kk = 0; kk < 10; ++kk) {
                const int k = hf * 10 + kk;
                const int r0 = kk * 32 + 8 * g + q4, r1 = r0 + 4, sw = (g & 1) << 5;
                LAS unsigned char* m0 = base + r0 * 64 + p4 * 8; LAS unsigned char* m1 = base + r1 * 64 + p4 * 8;
                bf16x8 yh0 = trfrag(m0 + (0 ^ sw), m1 + (0 ^ sw)), yh1 = trfrag(m0 + (32 ^ sw), m1 + (32 ^ sw));
                bf16x8 yl0 = trfrag(m0 + 20480 + (0 ^ sw), m1 + 20480 + (0 ^ sw)), yl1 = trfrag(m0 + 20480 + (32 ^ sw), m1 + 20480 + (32 ^ sw));
                a0 = __builtin_amdgcn_mfma_f32_16x16x32_bf16(__builtin_bit_cast(bf16x8, gl[k]), yh0, a0, 0, 0, 0);
                a1 = __builtin_amdgcn_mfma_f32_16x16x32_bf16(__builtin_bit_cast(bf16x8, gl[k]), yh1, a1, 0, 0, 0);
                a0 = __builtin_amdgcn_mfma_f32_16x16x32_bf16(__builtin_bit_cast(bf16x8, gh[k]), yl0, a0, 0, 0, 0);
                a1 = __builtin_amdgcn_mfma_f32_16x16x32_bf16(__builtin_bit_cast(bf16x8, gh[k]), yl1, a1, 0, 0, 0);
                a0 = __builtin_amdgcn_mfma_f32_16x16x32_bf16(__builtin_bit_cast(bf16x8, gh[k]), yh0, a0, 0, 0, 0);
                a1 = __builtin_amdgcn_mfma_f32_16x16x32_bf16(__builtin_bit_cast(bf16x8, gh[k]), yh1, a1, 0, 0, 0);
                if (kk & 1) __builtin_amdgcn_sched_barrier(0);
            }
            if (hf == 0) { lwrite(1); if (i + 1 < ns) gload(s + 1, 0); }
            else if (i + 1 < ns) { lwrite(0); gload(s + 1, 1); }
            __syncthreads();
        }
        const int d0 = db * 128 + w * 16 + 4 * g;
        v2u o0 = {pk2(a0[0] * sc, a0[1] * sc), pk2(a0[2] * sc, a0[3] * sc)}, o1 = {pk2(a1[0] * sc, a1[1] * sc), pk2(a1[2] * sc, a1[3] * sc)};
        __builtin_amdgcn_raw_buffer_store_b64(o0, rw, O_ABUF + (unsigned)((s * 32 + c) * D + d0) * 2u, 0, 16);
        __builtin_amdgcn_raw_buffer_store_b64(o1, rw, O_ABUF + (unsigned)((s * 32 + 16 + c) * D + d0) * 2u, 0, 16);
    }
}

__device__ __forceinline__ void task_wt2(const WS& ws, int tb, LAS unsigned char* lds) {
    const int tid = otid(), lane = tid & 63, w = __builtin_amdgcn_readfirstlane(tid >> 6);
    const rsrc_t rw = ws_rsrc(ws);
    LAS unsigned* tile = (LAS unsigned*)lds;
#pragma unroll
    for (int i = 0; i < 8; ++i) {
        const int e = w * 8 + i, tl = e >> 4, hf = (e >> 3) & 1, k2 = e & 7;
        const int p = (tb * 4 + tl) + 128 * hf, k0 = p >> 4, k1 = p & 15, u = k0 + 16 * k1 + 256 * k2;
#pragma unroll
        for (int q = 0; q < 6; ++q) {
            const int f = q * 64 + lane;
            if (f < DF) tile[f * 68 + e] = (unsigned)ws.wr[u * DF + f] | ((unsigned)ws.wi[u * DF + f] << 16);
        }
    }
    __syncthreads();
    for (int q = tid; q < DF * 16; q += 512) {
        const int f = q >> 4, ch = q & 15;
        st16(rw, O_WT + (unsigned)f * 8192 + tb * 256 + ch * 16, *(LAS v4u*)(tile + f * 68 + ch * 4));
    }
    __syncthreads();
}

template <int NTG>
__device__ __forceinline__ void task_attB2(const WS& ws, int s, int jg, int tg0, int f3c, LAS unsigned char* lds) {
    const int tid = otid(), lane = tid & 63, w = __builtin_amdgcn_readfirstlane(tid >> 6);
    const int g = lane >> 4, c = lane & 15, hh = c >> 3, t8 = c & 7;
    LAS unsigned char* px = lds; LAS unsigned char* pm = lds + 65536; LAS float* gate = (LAS float*)(lds + 81920);
    const rsrc_t rw = ws_rsrc(ws);
    const int j = jg * 8 + w;
    wait_ctr(rw, C_VW + s, 32); wait_ctr(rw, f3c, 5);
    v4u av[16]; v2u aa[NTG];
    {
        Pol pol; pol.m0 = ctr_get(rw, C_POL) - 1u; pol.m1 = ctr_get(rw, C_POL + 1) - 1u; pol.m2 = ctr_get(rw, C_POL + 2) - 1u;
        const unsigned vw = vw_base(pol, s, j) + lane * 16;
#pragma unroll
        for (int t2 = 0; t2 < 16; ++t2) av[t2] = ld16(rw, vw + vw_off(t2 * 1024));
#pragma unroll
        for (int tq = 0; tq < NTG; ++tq) aa[tq] = ld8(rw, O_ABUF + (unsigned)((s * 32 + (tg0 + tq) * 8 + t8) * D + j * 8 + (g & 1) * 4) * 2u);
        const int tok = tid >> 4, part = tid & 15;
        float acc = 0;
#pragma unroll
        for (int i = 0; i < 5; ++i) {
            v4u xv = *(const v4u*)(ws.x + (size_t)(s * 32 + tok) * D + (i * 16 + part) * 8), gv = *(const v4u*)(ws.gw + (i * 16 + part) * 8);
#pragma unroll
            for (int q = 0; q < 4; ++q) { unsigned pr = pk2(l16f(xv[q]) * l16f(gv[q]), h16f(xv[q]) * h16f(gv[q])); acc += l16f(pr) + h16f(pr); }
        }
#pragma unroll
        for (int o = 1; o < 16; o <<= 1) acc += __shfl_xor(acc, o);
        if (part == 0) { float z = rbf(acc); gate[tok] = rbf(1.0f / (1.0f + __expf(-z))); }
    }
    wait_ctr(rw, C_ATTA + s, 64);
    if (NTG == 4) {
        const unsigned src = O_PXF + (unsigned)s * 65536 + tid * 16;
        v4u t[8];
#pragma unroll
        for (int i = 0; i < 8; ++i) t[i] = ld16(rw, src + i * 8192);
        const unsigned src2 = O_PMF + (unsigned)s * 16384 + tid * 16;
        v4u u0 = ld16(rw, src2), u1 = ld16(rw, src2 + 8192);
#pragma unroll
        for (int i = 0; i < 8; ++i) *(LAS v4u*)(px + i * 8192 + tid * 16) = t[i];
        *(LAS v4u*)(pm + tid * 16) = u0; *(LAS v4u*)(pm + 8192 + tid * 16) = u1;
    } else {
        const int o0 = (((tid >> 5) * 2 + (tg0 >> 1)) * 64 + (tg0 & 1) * 8 + (tid & 7) + 16 * ((tid >> 3) & 3)) * 16;
        const int o2 = (((tid >> 3) & 31) * 32 + tg0 * 8 + (tid & 7)) * 16;
        v4u t0 = ld16(rw, O_PXF + (unsigned)s * 65536 + o0), t1 = ld16(rw, O_PXF + (unsigned)s * 65536 + o0 + 32768), u0 = ld16(rw, O_PMF + (unsigned)s * 16384 + o2);
        *(LAS v4u*)(px + o0) = t0; *(LAS v4u*)(px + o0 + 32768) = t1; *(LAS v4u*)(pm + o2) = u0;
    }
    __syncthreads();
    f32x4 acc[NTG], acc2[NTG];
#pragma unroll
    for (int tq = 0; tq < NTG; ++tq) { acc[tq] = (f32x4){0, 0, 0, 0}; acc2[tq] = (f32x4){0, 0, 0, 0}; }
    const bool early = ctr_get(rw, C_PROG) >= (unsigned)(s + 2);
    const unsigned uf = O_UF + (unsigned)(s * 80 + j) * 4096 + (2 * (g >> 1) + hh) * 128 + (g & 1) * 64 + t8 * 8;
    v2u au[8];
    if (early) {
#pragma unroll
        for (int t2 = 0; t2 < 8; ++t2) au[t2] = ld8(rw, uf + t2 * 512);
    }
#pragma unroll
    for (int t = 0; t < 16; ++t)
#pragma unroll
        for (int tq = 0; tq < NTG; ++tq) {
            const int tg = tg0 + tq;
            v4u bp = *(LAS v4u*)(px + (((2 * t + hh) * 2 + (tg >> 1)) * 64 + (tg & 1) * 8 + t8 + 16 * g) * 16);
            acc[tq] = __builtin_amdgcn_mfma_f32_16x16x32_bf16(__builtin_bit_cast(bf16x8, av[t]), __builtin_bit_cast(bf16x8, bp), acc[tq], 0, 0, 0);
        }
    if (!early) {
        wave_wait_ctr(rw, C_PROG, (unsigned)(s + 2));
#pragma unroll
        for (int t2 = 0; t2 < 8; ++t2) au[t2] = ld8(rw, uf + t2 * 512);
    }
#pragma unroll
    for (int t = 0; t < 8; ++t)
#pragma unroll
        for (int tq = 0; tq < NTG; ++tq) {
            const int tg = tg0 + tq;
            v2u bp = *(LAS v2u*)(pm + (((4 * t + 2 * (g >> 1) + hh) * 32 + tg * 8 + t8) * 16 + (g & 1) * 8));
            acc2[tq] = __builtin_amdgcn_mfma_f32_16x16x16bf16_1k(__builtin_bit_cast(s16x4, au[t]), __builtin_bit_cast(s16x4, bp), acc2[tq], 0, 0, 0);
        }
#pragma unroll
    for (int tq = 0; tq < NTG; ++tq) {
        const int tg = tg0 + tq;
        f32x4 o = acc[tq] + acc2[tq];
        float r4[4];
#pragma unroll
        for (int r = 0; r < 4; ++r) {
            unsigned hi = __builtin_amdgcn_permlane32_swap(__float_as_uint(o[r]), __float_as_uint(o[r]), false, false)[1];
            unsigned sh = __builtin_amdgcn_update_dpp(0u, hi, 0x108, 0xf, 0xf, false);
            r4[r] = o[r] + __uint_as_float(sh);
        }
        if (g < 2 && hh == 0) {
            const size_t off = (size_t)(s * 32 + tg * 8 + t8) * D + j * 8 + g * 4;
            const float gt = gate[tg * 8 + t8], om = rbf(1.0f - gt);
            const v2u av2 = aa[tq];
            unsigned b01 = pk2(r4[0], r4[1]), b23 = pk2(r4[2], r4[3]);
            unsigned ga01 = pk2(gt * l16f(av2.x), gt * h16f(av2.x)), ga23 = pk2(gt * l16f(av2.y), gt * h16f(av2.y));
            unsigned ob01 = pk2(om * l16f(b01), om * h16f(b01)), ob23 = pk2(om * l16f(b23), om * h16f(b23));
            v2u res = {pk2(l16f(ga01) + l16f(ob01), h16f(ga01) + h16f(ob01)), pk2(l16f(ga23) + l16f(ob23), h16f(ga23) + h16f(ob23))};
            *(v2u*)(ws.out + off) = res;
        }
    }
    __syncthreads();
}

__device__ __forceinline__ unsigned xcc_id() { return (unsigned)__builtin_amdgcn_s_getreg(20 | (3 << 11)) & 0xfu; }

constexpr int F1Q = 12, F3Q = 12, NF2FAR = 59;
constexpr int G_WVO = 0, G_DFT = 0, G_WT = G_DFT + 50, G_F1 = G_WT + 32, G_F2 = G_F1 + 5 * F1Q, G_F3 = G_F2 + NF2FAR, G_ATT = G_F3 + 5 * F3Q;
constexpr int ATT_PER = 10;
constexpr int NFINE = 2;
constexpr int G_FINE = G_ATT + (NSEG - NFINE) * ATT_PER, G_END = G_FINE + NFINE * ATT_PER * 4;
constexpr int NSTATIC = 82;

__device__ __forceinline__ void signal(rsrc_t rc, const WS& ws, unsigned ctr) {
    asm volatile("s_waitcnt vmcnt(0)" ::: "memory");
    __syncthreads();
    if (threadIdx.x == 0) ctr_add(ws, ctr);
}

__device__ __forceinline__ void worker(const WS& ws, int first, LAS unsigned char* lds, int maxit, int second) {
    const int tid = threadIdx.x;
    const rsrc_t rc = ws_rsrc(ws);
    LAS int* slot = (LAS int*)(lds + LDS_BYTES - 64);
    bool table = false;
    for (int iter = 0; iter < maxit; ++iter) {
        int t;
        if (second >= 0 && iter == 0 && first < 0) continue;
        if (iter == 0 && first >= 0) t = first;
        else if (second >= 0) t = second;
        else {
            if (tid == 0) { int q = NSTATIC + (int)ctr_add(ws, C_GHEAD); *slot = q < G_END ? q : -1; }
            __syncthreads();
            t = __builtin_amdgcn_readfirstlane(*slot);
            __syncthreads();
        }
        if (t < 0) break;
        if (t < G_WT) { for (int i = 0; i < 4; ++i) wave_dftmat(ws, ((t - G_DFT) * 4 + i) * 8 + (tid >> 6)); signal(rc, ws, C_DFT); }
        else if (t < G_F1) { task_wt2(ws, t - G_WT, lds); signal(rc, ws, C_WT); table = false; }
        else if (t < G_F2) {
            const int q = t - G_F1;
            wait_ctr(rc, C_DFT, 50);
            { const int p = q / 5, s0 = 64 * p / F1Q; task_f1s(ws, q % 5, s0, 64 * (p + 1) / F1Q - s0, lds); } signal(rc, ws, C_F1); table = false;
        } else if (t < G_F3 || t >= 50000) {
            wait_ctr(rc, C_F1, 5 * F1Q); wait_ctr(rc, C_WT, 32);
            task_f2(ws, t >= 50000 ? t - 50000 : t - G_F2, lds, !table); table = true; signal(rc, ws, C_F2);
        } else if (t < G_ATT) {
            const int q = t - G_F3;
            wait_ctr(rc, C_DFT, 50);
            { const int p = q / 5, s0 = 64 * p / F3Q; task_f3s(ws, q % 5, s0, 64 * (p + 1) / F3Q - s0, lds); } signal(rc, ws, C_F3 + q / 5); table = false;
        } else if (t < G_FINE) {
            const int q = t - G_ATT, s = q / ATT_PER, r = q % ATT_PER;
            task_attB2<4>(ws, s, r, 0, C_F3 + (F3Q * s + F3Q - 1) / 64, lds); table = false;
        } else {
            const int q = t - G_FINE, s = NSEG - NFINE + q / (ATT_PER * 4), r = q % (ATT_PER * 4);
            task_attB2<1>(ws, s, r >> 2, r & 3, C_F3 + (F3Q * s + F3Q - 1) / 64, lds); table = false;
        }
    }
}

__global__ void __launch_bounds__(512, 2) k_mega(WS ws, ChainCfg cfg) {
    extern __shared__ __attribute__((aligned(16))) unsigned char smem[];
    LAS unsigned char* lds = (LAS unsigned char*)smem;
    LAS int* slot = (LAS int*)(lds + LDS_BYTES - 64);
    const rsrc_t rc = ws_rsrc(ws);
    if (threadIdx.x == 0) {
        const unsigned x = xcc_id() & 7u;
        const unsigned t = ctr_add(ws, C_XCD + x);
        int sl = (int)(x * 32 + t);
        if (t >= 32) {
            const unsigned k = ctr_add(ws, C_OVF);
            unsigned spins = 0;
            for (;;) {
                unsigned tot = 0; for (int i = 0; i < 8; ++i) tot += ctr_get(rc, C_XCD + i);
                if (tot >= gridDim.x) break;
                __builtin_amdgcn_s_sleep(8); asm volatile("" ::: "memory");
                if (++spins > (1u << 22)) { set_err(rc, 9); break; }
            }
            unsigned acc = 0; sl = -1;
            for (int i = 0; i < 8; ++i) { const unsigned n = ctr_get(rc, C_XCD + i); const unsigned fr = n < 32 ? 32 - n : 0; if (sl < 0 && k < acc + fr) sl = i * 32 + (int)(n + (k - acc)); acc += fr; }
        }
        *slot = sl;
    }
    __syncthreads();
    const int sl = __builtin_amdgcn_readfirstlane(*slot);
    __syncthreads();
    if (sl < 0) return;
    const int x = sl >> 5, t = sl & 31;
    int first = -1, nearid = 0;
    if (x < 4) {
        if (t < 8) { head_role(ws, cfg, x * 8 + t, lds); return; }
        if (t < 16) { u_role(ws, cfg, x * 8 + (t - 8), lds); return; }
        if (t < 26) { st2_role(ws, cfg, x * 10 + (t - 16), lds); return; }
        if (sl == 31) { sched_role(ws, cfg, lds); return; }
        nearid = x * 6 + (t - 26) - (x > 0 ? 1 : 0);
        first = 64 + nearid;
    } else {
        if (t < 8) qk_role(ws, (x - 4) * 8 + t, lds);
        else if (t < 16) vo_role(ws, (x - 4) * 8 + (t - 8), lds);
        else first = (x - 4) * 16 + (t - 16);
    }
    if (first >= 0) {
        if (threadIdx.x < 250) { const unsigned v = __builtin_amdgcn_raw_buffer_load_b32(rc, (threadIdx.x << 20) + 65536u * (unsigned)(t & 15), 0, 16); asm volatile("" :: "v"(v)); }
        until_rt(rt32() + cfg.fl2);
    }
    if (first >= NSTATIC) first = -1;
    if (x < 4 && nearid >= 81 - NF2FAR) { if (first < 0) return; }
    worker(ws, first, lds, x < 4 ? (nearid < 81 - NF2FAR ? 2 : 1) : 1 << 30, (x < 4 && nearid < 81 - NF2FAR) ? 50000 + NF2FAR + nearid : -1);
}

extern "C" void kernel_launch(void* const* d_in, const int* in_sizes, int n_in, void* d_out, int out_size, void* d_ws, size_t ws_size, hipStream_t stream) {
    WS ws;
    ws.x = (const u16*)d_in[0]; ws.wr = (const u16*)d_in[1]; ws.wi = (const u16*)d_in[2]; ws.mem0 = (const u16*)d_in[4];
    ws.Wq = (const u16*)d_in[5]; ws.Wk = (const u16*)d_in[6]; ws.Wv = (const u16*)d_in[7]; ws.Wo = (const u16*)d_in[8]; ws.gw = (const u16*)d_in[10];
    ws.out = (u16*)d_out; ws.wsb = (unsigned char*)d_ws;
    (void)hipFuncSetAttribute((const void*)k_mega, hipFuncAttributeMaxDynamicSharedMemorySize, LDS_BYTES);
    (void)hipMemsetAsync(d_ws, 0xFF, CTL_END, stream);
#define CFG_T1 86
#define CFG_T2 80
#define CFG_T3 140
#define CFG_PAD 120
#define CFG_D0 -6
#define CFG_PEN 28
#define CFG_SD1 60
#define CFG_SD2 8
#define CFG_FL 5
#define CFG_M2 2
    ChainCfg cfg{CFG_T1, CFG_T2, CFG_T3, CFG_PAD, CFG_D0, CFG_PEN, CFG_SD1, CFG_SD2, CFG_FL, 1000, CFG_M2, 0};
    k_mega<<<256, 512, LDS_BYTES, stream>>>(ws, cfg);
}
```
